```python
import torch

def _softmax(x, axis=-1):
    return torch.softmax(x, dim=axis)
def _gelu(x):
    return torch.nn.functional.gelu(x, approximate="tanh")
def _silu(x):
    return torch.nn.functional.silu(x)
def _sigmoid(x):
    return torch.sigmoid(x)
def _layernorm(x, axis=-1, eps=1e-5):
    m = x.mean(dim=axis, keepdim=True)
    v = x.var(dim=axis, keepdim=True, unbiased=False)
    return (x - m) / torch.sqrt(v + eps)
def _scatter_add(tgt, idx, src, axis=0):
    return tgt.clone().index_add_(axis, idx, src)
def _segment_sum(src, seg, n):
    out = torch.zeros((n,) + src.shape[1:], dtype=src.dtype)
    return out.index_add_(0, seg, src)
def _pad(x, pw):
    flat = tuple(v for pair in reversed(pw) for v in pair)
    return torch.nn.functional.pad(x, flat)

kernel_name = 'parallel_sum__sliding_block_cached__glu_gated_swiglu__decode'

def setup_inputs():

    _w = (lambda a: (lambda t: t.to(torch.bfloat16) if t.is_floating_point() else t)(torch.as_tensor(a)))
    def _A_setup():
        B, Sq, D = 4, 1, 640
        Hq, Hkv, Sk, Dh = 32, 32, 8192, 192
        assert Hq % Hkv == 0, (Hq, Hkv)
        return {
            "x": _w((lambda *s: torch.randn(*s, dtype=torch.bfloat16) * 0.05)(B, Sq, D)),
            "k_cache": _w((lambda *s: torch.randn(*s, dtype=torch.bfloat16) * 1.6)(B, Hkv, Sk, Dh)),
            "v_cache": _w((lambda *s: torch.randn(*s, dtype=torch.bfloat16) * 0.8)(B, Hkv, Sk, Dh)),
            "wq": _w((lambda *s: torch.randn(*s, dtype=torch.bfloat16) * 1.6)(D, Hq * Dh) * (D ** -0.5)),
            "wo": _w((lambda *s: torch.randn(*s, dtype=torch.bfloat16) * 0.8)(Hq * Dh, D) * ((Hq * Dh) ** -0.5)),
        }
    _A_in = _A_setup()

    def _B_setup():
        B, S, D, F = 4, 1, 640, 1536
        return {
            "x": _w((lambda *s: torch.randn(*s, dtype=torch.bfloat16) * 0.05)(B, S, D)),
            "w1": _w((lambda *s: torch.randn(*s, dtype=torch.bfloat16) * 0.4)(D, F) * (2.0 / D) ** 0.5),
            "w3": _w((lambda *s: torch.randn(*s, dtype=torch.bfloat16) * 0.4)(D, F) * (2.0 / D) ** 0.5),
            "w2": _w((lambda *s: torch.randn(*s, dtype=torch.bfloat16) * 0.4)(F, D) * (2.0 / F) ** 0.5),
        }
    _B_in = _B_setup()

    return {
        "A_x": _A_in["x"],
        "A_k_cache": _A_in["k_cache"],
        "A_v_cache": _A_in["v_cache"],
        "A_wq": _A_in["wq"],
        "A_wo": _A_in["wo"],
        "B_x": _B_in["x"],
        "B_w1": _B_in["w1"],
        "B_w3": _B_in["w3"],
        "B_w2": _B_in["w2"],
    }

def reference(A_x, A_k_cache, A_v_cache, A_wq, A_wo, B_x, B_w1, B_w3, B_w2):
    def _A_ref(x, k_cache, v_cache, wq, wo):
        B, Sq, D = x.shape
        Hq, Dh = 32, 192
        Hkv, Sk = k_cache.shape[1], k_cache.shape[2]
        W = min(4096, max(256, Sk // 4))
        rep = Hq // Hkv
        q = (lambda x, *d: x.permute(*d))((x @ wq).reshape(B, Sq, Hq, Dh), 0, 2, 1, 3)
        k = (lambda x, n, axis: torch.repeat_interleave(x, n, dim=axis))(k_cache[:, :, Sk - W:, :], rep, axis=1)
        v = (lambda x, n, axis: torch.repeat_interleave(x, n, dim=axis))(v_cache[:, :, Sk - W:, :], rep, axis=1)
        scores = torch.einsum("bhqd,bhkd->bhqk", q, k) * (Dh ** -0.5)
        p = _softmax(scores, axis=-1)
        o = torch.einsum("bhqk,bhkd->bhqd", p, v)
        return (lambda x, *d: x.permute(*d))(o, 0, 2, 1, 3).reshape(B, Sq, Hq * Dh) @ wo

    def _B_ref(x, w1, w3, w2):
        gate = torch.einsum("bsd,df->bsf", x, w1)
        up = torch.einsum("bsd,df->bsf", x, w3)
        h = _silu(gate) * up
        out = torch.einsum("bsf,fd->bsd", h, w2)
        return out

    x = A_x
    xn = _layernorm(x, axis=-1)
    return x + _A_ref(xn, A_k_cache, A_v_cache, A_wq, A_wo) + _B_ref(xn, B_w1, B_w3, B_w2)
```

```cpp
#include <hip/hip_runtime.h>
#include <hip/hip_bf16.h>
#include <cstdint>

using bf16 = __hip_bfloat16;
using u32x2 = __attribute__((ext_vector_type(2))) unsigned;
using u32x4 = __attribute__((ext_vector_type(4))) unsigned;
using f32x2 = __attribute__((ext_vector_type(2))) float;
using f32x4 = __attribute__((ext_vector_type(4))) float;
using bf16x2 = __attribute__((ext_vector_type(2))) __bf16;
using bf16x8 = __attribute__((ext_vector_type(8))) __bf16;
typedef unsigned long long u64;

constexpr int NB = 4, DM = 640, NH = 32, DH = 192, SK = 8192, WIN = 2048, FF = 1536;
constexpr float LOG2E = 1.4426950408889634f;
constexpr float SCALE = 0.07216878364870322f;
constexpr int WINL = 4;
constexpr int WINV = 12;
constexpr int TSW = 24;
constexpr int QT = 570;
constexpr int QBACK = 16;
constexpr int TAU = 86;
constexpr int TK = 11;
constexpr int TAU2 = 160;
constexpr int PR1 = 60;
constexpr int PR2 = 150;
constexpr int PR3 = 300;
constexpr int PRS = 3;
constexpr int CPE = 1;
constexpr int CPL = 1;
constexpr int CODEB = 29504;
constexpr int PT1 = 195;
constexpr int ET = 5;
constexpr int SPAN = 80;
constexpr int TOFS = 116;
constexpr int RING = 9;
constexpr int WTRIG = 31;
constexpr int LATE = 100;
constexpr int WINB = 8;
constexpr int MLD = 300;
constexpr int PSL1 = 6;
constexpr int RSH = 3;
constexpr int CAL = 5;
constexpr int LG = 4;
constexpr int WT = 78;
constexpr int FW1 = 1;
constexpr int WTF = 122;
constexpr int NCK = 3;
constexpr int CK0 = 7;
constexpr int CKL = 18;
constexpr int CKS = 6;
constexpr int CG0 = 36;
constexpr int CZ0 = 30;
constexpr int CG1 = 30;
constexpr int CZ1 = 20;
constexpr int MA = 22;
constexpr int MZ = 18;
constexpr int MB = 200;
constexpr int MY = 27;
constexpr int MC = 60;
constexpr int MD = 10;
constexpr int CRD = 110;
constexpr int YP = 8;
constexpr int YL = 256;
constexpr int YLO = 99u;
constexpr int YHI = 139u;
constexpr int WB = 144;
constexpr int WPH = 44;
constexpr int WPB = 50;
constexpr int PD = 52;
constexpr int CKO = 35;
constexpr int RG = 8;
constexpr int R0 = 415;
constexpr int RMX = 120;
constexpr int LGN = 12;
constexpr int L0 = 75;
constexpr int NREP = 2;
constexpr int MLS = 16;
constexpr int MLH = 256;
constexpr int FGT = 1010;
constexpr int TREP = 2;
constexpr int TCK = 1;
constexpr int TYM = 15;
constexpr int TWM = 15;
constexpr int TOM = 7;
constexpr int RPM = 4;
constexpr int CP0 = 100;
constexpr int CP1 = 60;
constexpr int CP2 = 45;
constexpr int CUP = 65;
constexpr int SLZ = 29;
constexpr int SLD = 5;
constexpr int SLM = 4;
constexpr int SLY = 18;
constexpr int TGT = 28;
constexpr int TGG = 10;
constexpr int NPH = 20;
constexpr int WPF = 22;
constexpr int SOFS = 30;
constexpr int UID = 0;
constexpr int PF = 4;
constexpr int ALT = 0;
constexpr int AO = 0;
constexpr int ZE = 0;
constexpr int TOL = 0;
constexpr int RB = 0;
constexpr int HQX = 5;
constexpr int sched(int t) { return t <= TK ? TAU * t : TAU * TK + ((TAU2 * (t - TK)) >> 1) + (t >= TGT ? TGG : 0); }
constexpr int viss0(int v) { return v <= 2 * (RING - 1 - LG) + 1 ? ((v & 1) ? 16 + RING + (v >> 1) : 0) : 16 + v + LG; }
constexpr int viss(int v) { return viss0(v) < 32 ? viss0(v) : 0; }
constexpr int PCAP = 150;
constexpr int ckt(int k) { return k == NCK - 1 ? CKL : CK0 + k * CKS; }
constexpr int ckof(int t) { for (int k = 0; k < NCK; ++k) if (t == ckt(k) || t == ckt(k) + 1) return k; return -1; }
constexpr int NBUF = RING;
constexpr int NT = 16;
constexpr int TSTR = 4 * 8 * 8 * DH * 2;
constexpr int BSTR = 2 * NH * SK * DH * 2;

constexpr int WS_EPOCH = 0;
constexpr int WS_Q = 512;
constexpr int WS_GU = WS_Q + 2 * NH * DH * 8;
constexpr int WS_ML = WS_GU + 2 * 2 * FF * 8;
constexpr int WS_TM = WS_ML + NH * MLH;
constexpr int WS_CK = WS_TM + 8 * 128;
constexpr int WS_O = WS_CK + 4 * 8 * 1024;
constexpr int NY = NH + 16;
constexpr int WS_Y = (WS_O + NH * NB * 4 * DH * 4 + 65535) & ~65535;
constexpr int WS_S = WS_Y + (NB * DM / YP) * YL;
constexpr int WS_END = WS_S + NY * NB * DM * 8;

__device__ __forceinline__ float dot2(unsigned k, unsigned q, float acc) {
  return __builtin_amdgcn_fdot2_f32_bf16(__builtin_bit_cast(bf16x2, k), __builtin_bit_cast(bf16x2, q), acc, false);
}
__device__ __forceinline__ float bflo(unsigned v) { return __builtin_bit_cast(float, v << 16); }
__device__ __forceinline__ float bfhi(unsigned v) { return __builtin_bit_cast(float, v & 0xffff0000u); }
template <int CTRL> __device__ __forceinline__ float dpp(float x) {
  return __builtin_bit_cast(float, __builtin_amdgcn_mov_dpp(__builtin_bit_cast(int, x), CTRL, 0xf, 0xf, true));
}
constexpr int XOR1 = 0xB1, XOR2 = 0x4E, XOR7 = 0x141, XOR8 = 0x128;
__device__ __forceinline__ float x16sum(float x) {
  auto s = __builtin_amdgcn_permlane16_swap(__float_as_uint(x), __float_as_uint(x), false, false);
  return __uint_as_float(s[0]) + __uint_as_float(s[1]);
}
__device__ __forceinline__ float x32sum(float x) {
  auto t = __builtin_amdgcn_permlane32_swap(__float_as_uint(x), __float_as_uint(x), false, false);
  return __uint_as_float(t[0]) + __uint_as_float(t[1]);
}
__device__ __forceinline__ float x16max(float x) {
  auto s = __builtin_amdgcn_permlane16_swap(__float_as_uint(x), __float_as_uint(x), false, false);
  return fmaxf(__uint_as_float(s[0]), __uint_as_float(s[1]));
}
__device__ __forceinline__ float x32max(float x) {
  auto t = __builtin_amdgcn_permlane32_swap(__float_as_uint(x), __float_as_uint(x), false, false);
  return fmaxf(__uint_as_float(t[0]), __uint_as_float(t[1]));
}
__device__ __forceinline__ float sum_kk(float x) { x += dpp<XOR8>(x); x = x16sum(x); return x32sum(x); }
__device__ __forceinline__ float wave_sum(float x) {
  x += dpp<XOR1>(x); x += dpp<XOR2>(x); x += dpp<XOR7>(x); return sum_kk(x);
}
__device__ __forceinline__ float tr32(float a, float b) {
  auto t = __builtin_amdgcn_permlane32_swap(__float_as_uint(a), __float_as_uint(b), false, false);
  return __uint_as_float(t[0]) + __uint_as_float(t[1]);
}
__device__ __forceinline__ float tr16(float a, float b) {
  auto t = __builtin_amdgcn_permlane16_swap(__float_as_uint(a), __float_as_uint(b), false, false);
  return __uint_as_float(t[0]) + __uint_as_float(t[1]);
}
__device__ __forceinline__ unsigned cvtpk(float lo, float hi) {
  return __builtin_bit_cast(unsigned, __builtin_convertvector(f32x2{lo, hi}, bf16x2));
}
__device__ __forceinline__ float rbf(float v) { return bflo(cvtpk(v, 0.f)); }
__device__ __forceinline__ f32x4 mfma(u32x4 a, u32x4 b, f32x4 c) {
  return __builtin_amdgcn_mfma_f32_16x16x32_bf16(__builtin_bit_cast(bf16x8, a), __builtin_bit_cast(bf16x8, b), c, 0, 0, 0);
}
__device__ __forceinline__ u32x4 bfrag(const u32x4* r, int ec) {
  u32x4 f;
#pragma unroll
  for (int p = 0; p < 4; ++p) f[p] = __builtin_amdgcn_perm(r[2 * p + 1][ec >> 1], r[2 * p][ec >> 1], (ec & 1) ? 0x07060302u : 0x05040100u);
  return f;
}
typedef __amdgpu_buffer_rsrc_t rsrc_t;
__device__ __forceinline__ rsrc_t mkrs(const void* p, unsigned n) { return __builtin_amdgcn_make_buffer_rsrc(const_cast<void*>(p), (short)0, (int)n, 0x00020000); }
constexpr int AUX_NT = 2, AUX_SC1 = 16;
__device__ __forceinline__ u32x4 ldb(rsrc_t rs, int voff, int soff) { return __builtin_amdgcn_raw_buffer_load_b128(rs, voff, soff, 0); }
__device__ __forceinline__ u32x4 ldb_nt(rsrc_t rs, int voff, int soff) { return __builtin_amdgcn_raw_buffer_load_b128(rs, voff, soff, AUX_NT); }
__device__ __forceinline__ u32x4 ld_ag(rsrc_t rs, int voff) { return __builtin_amdgcn_raw_buffer_load_b128(rs, voff, 0, AUX_SC1); }
__device__ __forceinline__ void st_ag(rsrc_t rs, int voff, u32x4 v) { __builtin_amdgcn_raw_buffer_store_b128(v, rs, voff, 0, AUX_SC1); }
struct Tag { unsigned t[4]; };
__device__ __forceinline__ Tag mktag(unsigned e) { return Tag{{e & 1023u, (e + 2 * (e >> 10)) & 1023u, (e + 0x156u) & 1023u, (e + 2 * (e >> 19) + 0x2a8u) & 1023u}}; }
__device__ __forceinline__ unsigned r22(float v) { return (__float_as_uint(v) + 0x200u) & ~1023u; }
__device__ __forceinline__ float rnd22(float v) { return __uint_as_float(r22(v)); }
__device__ __forceinline__ u32x4 g_pack(float a, float b, float c, float d, Tag t) {
  return u32x4{r22(a) | t.t[0], r22(b) | t.t[1], r22(c) | t.t[2], r22(d) | t.t[3]};
}
__device__ __forceinline__ u32x4 g_int(unsigned a, unsigned c, Tag t) { return u32x4{(a << 10) | t.t[0], t.t[1], (c << 10) | t.t[2], t.t[3]}; }
__device__ __forceinline__ bool g_ok(u32x4 g, Tag t) { return (((g[0] ^ t.t[0]) | (g[1] ^ t.t[1]) | (g[2] ^ t.t[2]) | (g[3] ^ t.t[3])) & 1023u) == 0u; }
__device__ __forceinline__ f32x4 g_val(u32x4 g, Tag t) {
  return f32x4{__uint_as_float(g[0] ^ t.t[0]), __uint_as_float(g[1] ^ t.t[1]), __uint_as_float(g[2] ^ t.t[2]), __uint_as_float(g[3] ^ t.t[3])};
}
__device__ __forceinline__ u32x4 s_tag(unsigned e, int lane) {
  u32x4 t;
#pragma unroll
  for (int i = 0; i < 4; ++i) t[i] = (e + 2 * (e >> (4 + 7 * i + 2 * (lane & 3)))) & 15u;
  return t;
}
__device__ __forceinline__ u32x2 s_pack(f32x4 v, unsigned t0, unsigned t1) {
  const unsigned eb = max(max(__float_as_uint(v[0]) << 1, __float_as_uint(v[1]) << 1), max(__float_as_uint(v[2]) << 1, __float_as_uint(v[3]) << 1)) >> 24;
  unsigned n[4];
#pragma unroll
  for (int i = 0; i < 4; ++i) n[i] = (unsigned)min((int)rintf(__builtin_amdgcn_ldexpf(v[i], 137 - (int)eb)), 2047);
  return u32x2{(n[0] << 20) | ((n[2] & 4095u) << 8) | ((eb & 15u) << 4) | t0, (n[1] << 20) | ((n[3] & 4095u) << 8) | (eb & 240u) | t1};
}
__device__ __forceinline__ unsigned s_dif(u32x4 g, u32x4 t) { return (g[0] ^ t[0]) | (g[1] ^ t[1]) | (g[2] ^ t[2]) | (g[3] ^ t[3]); }
__device__ __forceinline__ float s_scale(unsigned a, unsigned b) { return __uint_as_float(((a & 240u) << 19) | ((b & 240u) << 23)) * (1.f / 1024); }
__device__ __forceinline__ float s_hi(unsigned a) { return (float)((int)a >> 20); }
__device__ __forceinline__ float s_lo(unsigned a) { return (float)((int)(a << 12) >> 20); }
constexpr int SPIN_MAX = 1 << 18;
#define SB() __builtin_amdgcn_sched_barrier(0)
#define FENCE() asm volatile("" ::: "memory")
#define BAR() asm volatile("s_waitcnt lgkmcnt(0)\n\ts_barrier" ::: "memory")
#define VMW(n) asm volatile("s_waitcnt vmcnt(%0)" :: "n"(n) : "memory")
template <int SL, int FW> __device__ __forceinline__ void poll4(f32x4 (&r)[4], u32x4 (&p)[12], const int (&f)[4], rsrc_t rs, Tag tag) {
  unsigned t, u, z, cnt;
  asm volatile(
      "s_mov_b32 %[cnt], %[mx]\n\t"
      ".if %[fw]\n\t"
      "buffer_load_dwordx4 v[64:67], %[f0], %[rs], 0 offen sc1\n\tbuffer_load_dwordx4 v[68:71], %[f1], %[rs], 0 offen sc1\n\tbuffer_load_dwordx4 v[72:75], %[f2], %[rs], 0 offen sc1\n\tbuffer_load_dwordx4 v[76:79], %[f3], %[rs], 0 offen sc1\n\t"
      "s_waitcnt vmcnt(0)\n\tv_xor_b32 v112, %[t0], v64\n\tv_xor_b32 v113, %[t1], v65\n\tv_xor_b32 v114, %[t2], v66\n\tv_xor_b32 v115, %[t3], v67\n\tv_xor_b32 v116, %[t0], v68\n\tv_xor_b32 v117, %[t1], v69\n\tv_xor_b32 v118, %[t2], v70\n\tv_xor_b32 v119, %[t3], v71\n\tv_xor_b32 v120, %[t0], v72\n\tv_xor_b32 v121, %[t1], v73\n\tv_xor_b32 v122, %[t2], v74\n\tv_xor_b32 v123, %[t3], v75\n\tv_xor_b32 v124, %[t0], v76\n\tv_xor_b32 v125, %[t1], v77\n\tv_xor_b32 v126, %[t2], v78\n\tv_xor_b32 v127, %[t3], v79\n\tv_or3_b32 %[t], v112, v113, v114\n\tv_or3_b32 %[u], v115, v116, v117\n\tv_or3_b32 %[z], v118, v119, v120\n\tv_or3_b32 %[t], %[t], v121, v122\n\tv_or3_b32 %[u], %[u], v123, v124\n\tv_or3_b32 %[z], %[z], v125, v126\n\tv_or3_b32 %[t], %[t], %[u], %[z]\n\tv_or_b32 %[t], %[t], v127\n\tv_lshlrev_b32 %[t], 22, %[t]\n\tv_cmp_ne_u32 vcc, 0, %[t]\n\ts_mov_b64 exec, vcc\n\ts_cbranch_execz 2%=f\n\t"
      ".endif\n\t"
      "buffer_load_dwordx4 v[64:67], %[f0], %[rs], 0 offen sc1\n\tbuffer_load_dwordx4 v[68:71], %[f1], %[rs], 0 offen sc1\n\tbuffer_load_dwordx4 v[72:75], %[f2], %[rs], 0 offen sc1\n\tbuffer_load_dwordx4 v[76:79], %[f3], %[rs], 0 offen sc1\n\ts_sleep %[sl]\n\t"
      "buffer_load_dwordx4 v[80:83], %[f0], %[rs], 0 offen sc1\n\tbuffer_load_dwordx4 v[84:87], %[f1], %[rs], 0 offen sc1\n\tbuffer_load_dwordx4 v[88:91], %[f2], %[rs], 0 offen sc1\n\tbuffer_load_dwordx4 v[92:95], %[f3], %[rs], 0 offen sc1\n\ts_sleep %[sl]\n\t"
      "1%=:\n\t"
      "buffer_load_dwordx4 v[96:99], %[f0], %[rs], 0 offen sc1\n\tbuffer_load_dwordx4 v[100:103], %[f1], %[rs], 0 offen sc1\n\tbuffer_load_dwordx4 v[104:107], %[f2], %[rs], 0 offen sc1\n\tbuffer_load_dwordx4 v[108:111], %[f3], %[rs], 0 offen sc1\n\t"
      "s_waitcnt vmcnt(8)\n\tv_xor_b32 v112, %[t0], v64\n\tv_xor_b32 v113, %[t1], v65\n\tv_xor_b32 v114, %[t2], v66\n\tv_xor_b32 v115, %[t3], v67\n\tv_xor_b32 v116, %[t0], v68\n\tv_xor_b32 v117, %[t1], v69\n\tv_xor_b32 v118, %[t2], v70\n\tv_xor_b32 v119, %[t3], v71\n\tv_xor_b32 v120, %[t0], v72\n\tv_xor_b32 v121, %[t1], v73\n\tv_xor_b32 v122, %[t2], v74\n\tv_xor_b32 v123, %[t3], v75\n\tv_xor_b32 v124, %[t0], v76\n\tv_xor_b32 v125, %[t1], v77\n\tv_xor_b32 v126, %[t2], v78\n\tv_xor_b32 v127, %[t3], v79\n\tv_or3_b32 %[t], v112, v113, v114\n\tv_or3_b32 %[u], v115, v116, v117\n\tv_or3_b32 %[z], v118, v119, v120\n\tv_or3_b32 %[t], %[t], v121, v122\n\tv_or3_b32 %[u], %[u], v123, v124\n\tv_or3_b32 %[z], %[z], v125, v126\n\tv_or3_b32 %[t], %[t], %[u], %[z]\n\tv_or_b32 %[t], %[t], v127\n\tv_lshlrev_b32 %[t], 22, %[t]\n\tv_cmp_ne_u32 vcc, 0, %[t]\n\ts_mov_b64 exec, vcc\n\ts_cbranch_execz 2%=f\n\t"
      "buffer_load_dwordx4 v[64:67], %[f0], %[rs], 0 offen sc1\n\tbuffer_load_dwordx4 v[68:71], %[f1], %[rs], 0 offen sc1\n\tbuffer_load_dwordx4 v[72:75], %[f2], %[rs], 0 offen sc1\n\tbuffer_load_dwordx4 v[76:79], %[f3], %[rs], 0 offen sc1\n\t"
      "s_waitcnt vmcnt(8)\n\tv_xor_b32 v112, %[t0], v80\n\tv_xor_b32 v113, %[t1], v81\n\tv_xor_b32 v114, %[t2], v82\n\tv_xor_b32 v115, %[t3], v83\n\tv_xor_b32 v116, %[t0], v84\n\tv_xor_b32 v117, %[t1], v85\n\tv_xor_b32 v118, %[t2], v86\n\tv_xor_b32 v119, %[t3], v87\n\tv_xor_b32 v120, %[t0], v88\n\tv_xor_b32 v121, %[t1], v89\n\tv_xor_b32 v122, %[t2], v90\n\tv_xor_b32 v123, %[t3], v91\n\tv_xor_b32 v124, %[t0], v92\n\tv_xor_b32 v125, %[t1], v93\n\tv_xor_b32 v126, %[t2], v94\n\tv_xor_b32 v127, %[t3], v95\n\tv_or3_b32 %[t], v112, v113, v114\n\tv_or3_b32 %[u], v115, v116, v117\n\tv_or3_b32 %[z], v118, v119, v120\n\tv_or3_b32 %[t], %[t], v121, v122\n\tv_or3_b32 %[u], %[u], v123, v124\n\tv_or3_b32 %[z], %[z], v125, v126\n\tv_or3_b32 %[t], %[t], %[u], %[z]\n\tv_or_b32 %[t], %[t], v127\n\tv_lshlrev_b32 %[t], 22, %[t]\n\tv_cmp_ne_u32 vcc, 0, %[t]\n\ts_mov_b64 exec, vcc\n\ts_cbranch_execz 2%=f\n\t"
      "buffer_load_dwordx4 v[80:83], %[f0], %[rs], 0 offen sc1\n\tbuffer_load_dwordx4 v[84:87], %[f1], %[rs], 0 offen sc1\n\tbuffer_load_dwordx4 v[88:91], %[f2], %[rs], 0 offen sc1\n\tbuffer_load_dwordx4 v[92:95], %[f3], %[rs], 0 offen sc1\n\t"
      "s_waitcnt vmcnt(8)\n\tv_xor_b32 v112, %[t0], v96\n\tv_xor_b32 v113, %[t1], v97\n\tv_xor_b32 v114, %[t2], v98\n\tv_xor_b32 v115, %[t3], v99\n\tv_xor_b32 v116, %[t0], v100\n\tv_xor_b32 v117, %[t1], v101\n\tv_xor_b32 v118, %[t2], v102\n\tv_xor_b32 v119, %[t3], v103\n\tv_xor_b32 v120, %[t0], v104\n\tv_xor_b32 v121, %[t1], v105\n\tv_xor_b32 v122, %[t2], v106\n\tv_xor_b32 v123, %[t3], v107\n\tv_xor_b32 v124, %[t0], v108\n\tv_xor_b32 v125, %[t1], v109\n\tv_xor_b32 v126, %[t2], v110\n\tv_xor_b32 v127, %[t3], v111\n\tv_or3_b32 %[t], v112, v113, v114\n\tv_or3_b32 %[u], v115, v116, v117\n\tv_or3_b32 %[z], v118, v119, v120\n\tv_or3_b32 %[t], %[t], v121, v122\n\tv_or3_b32 %[u], %[u], v123, v124\n\tv_or3_b32 %[z], %[z], v125, v126\n\tv_or3_b32 %[t], %[t], %[u], %[z]\n\tv_or_b32 %[t], %[t], v127\n\tv_lshlrev_b32 %[t], 22, %[t]\n\tv_cmp_ne_u32 vcc, 0, %[t]\n\ts_mov_b64 exec, vcc\n\ts_cbranch_execz 2%=f\n\t"
      "s_sub_u32 %[cnt], %[cnt], 1\n\ts_cmp_lg_u32 %[cnt], 0\n\ts_cbranch_scc1 1%=b\n2%=:\n\ts_mov_b64 exec, -1"
      : "=&{v[112:115]}"(r[0]), "=&{v[116:119]}"(r[1]), "=&{v[120:123]}"(r[2]), "=&{v[124:127]}"(r[3]),
        "=&{v[64:67]}"(p[0]), "=&{v[68:71]}"(p[1]), "=&{v[72:75]}"(p[2]), "=&{v[76:79]}"(p[3]), "=&{v[80:83]}"(p[4]), "=&{v[84:87]}"(p[5]), "=&{v[88:91]}"(p[6]), "=&{v[92:95]}"(p[7]), "=&{v[96:99]}"(p[8]), "=&{v[100:103]}"(p[9]), "=&{v[104:107]}"(p[10]), "=&{v[108:111]}"(p[11]),
        [t] "=&v"(t), [u] "=&v"(u), [z] "=&v"(z), [cnt] "=&s"(cnt)
      : [f0] "v"(f[0]), [f1] "v"(f[1]), [f2] "v"(f[2]), [f3] "v"(f[3]), [rs] "s"(rs), [t0] "s"(tag.t[0]), [t1] "s"(tag.t[1]), [t2] "s"(tag.t[2]), [t3] "s"(tag.t[3]), [mx] "n"(SPIN_MAX), [sl] "n"(SL), [fw] "n"(FW)
      : "vcc", "scc", "memory");
}
__device__ __forceinline__ void poll4_keep(u32x4 (&p)[12]) { asm volatile("" :: "{v[64:67]}"(p[0]), "{v[68:71]}"(p[1]), "{v[72:75]}"(p[2]), "{v[76:79]}"(p[3]), "{v[80:83]}"(p[4]), "{v[84:87]}"(p[5]), "{v[88:91]}"(p[6]), "{v[92:95]}"(p[7]), "{v[96:99]}"(p[8]), "{v[100:103]}"(p[9]), "{v[104:107]}"(p[10]), "{v[108:111]}"(p[11])); }
#define LDSWAITV(fs_, word, n, slp) do { unsigned fv_; \
    asm volatile("1%=:\n\tds_read_b32 %0, %2\n\ts_waitcnt lgkmcnt(0)\n\tv_readfirstlane_b32 %1, %0\n\ts_cmp_lt_u32 %1, %3\n\ts_cbranch_scc0 2%=f\n\ts_sleep %4\n\ts_branch 1%=b\n2%=:" \
                 : "=&v"(fv_), "=&s"(fs_) : "v"((unsigned)(uintptr_t)&(word)), "n"(n), "n"(slp) : "scc", "memory"); } while (0)
#define LDSINC(word) asm volatile("s_mov_b64 exec, 1\n\tds_add_u32 %0, %1\n\ts_mov_b64 exec, -1" :: "v"((unsigned)(uintptr_t)&(word)), "v"(1u) : "memory")
#define LDSWAIT(word, n, slp) do { unsigned fs0_; LDSWAITV(fs0_, word, n, slp); } while (0)

#define LAND(n) asm volatile(".Lland%0_" #n ":\n\ts_cbranch_execz .Lback%0" :: "n"(UID))
constexpr int ASTR = 132;

__global__ void __attribute__((aligned(32768))) __launch_bounds__(768, 3)
fused_k(const bf16* __restrict__ x, const bf16* __restrict__ kc, const bf16* __restrict__ vc, const bf16* __restrict__ wq,
        const bf16* __restrict__ wo, const bf16* __restrict__ w1, const bf16* __restrict__ w3, const bf16* __restrict__ w2,
        unsigned char* __restrict__ ws, bf16* __restrict__ out) {
  __shared__ __attribute__((aligned(16))) f32x4 red[16][8][9];
  __shared__ __attribute__((aligned(16))) float stat[NB][2];
  __shared__ __attribute__((aligned(16))) unsigned qs[2][DH / 2];
  __shared__ __attribute__((aligned(16))) unsigned clk[8];
  __shared__ __attribute__((aligned(16))) unsigned ctl[20];
  __shared__ __attribute__((aligned(16))) float mrg[2][8][DH];
  __shared__ __attribute__((aligned(16))) float mlw[3][16];
  __shared__ __attribute__((aligned(16))) unsigned oA[NB][ASTR];
  __shared__ __attribute__((aligned(16))) u32x4 wf[6][8][64];
  asm volatile("s_cmp_gt_u32 %1, 255\n\ts_cbranch_scc1 .Lback%0\n\ts_mov_b64 exec, 0\n\ts_cmp_lt_u32 %1, 64\n\ts_cbranch_scc1 .Lland%0_0\n\t"
               "s_cmpk_lt_u32 %1, 128\n\ts_cbranch_scc1 .Lland%0_1\n\ts_cmpk_lt_u32 %1, 192\n\ts_cbranch_scc1 .Lland%0_2\n\ts_branch .Lland%0_3\n"
               ".Lback%0:\n\ts_mov_b64 exec, -1" :: "n"(UID), "s"(__builtin_amdgcn_readfirstlane(threadIdx.x)) : "scc");
  SB();
  u64 pc0;
  asm volatile("s_getpc_b64 %0" : "=s"(pc0));
  asm volatile("" :: "s"(x), "s"(kc), "s"(vc), "s"(wq), "s"(wo), "s"(w1), "s"(w3), "s"(w2), "s"(ws), "s"(out));
#define HOF(b) (ALT == 1 ? ((b) & 24) | (((b) & 3) << 1) | (((b) >> 2) & 1) : ALT == 2 ? ((b) & 25) | (((b) & 2) << 1) | (((b) >> 1) & 2) : ALT == 3 ? ((b) & 31) ^ 4 : (b) & 31)
#define ROF(h) (RB == 1 ? ((h) >= 18 && (h) < 22 ? (h) ^ 6 : (h) == 27 || (h) == 30 ? (h) ^ 5 : (h)) : RB == 2 ? ((h) >= 16 && (h) < 24 ? (h) ^ 4 : (h)) : (h))
  const int h = HOF(blockIdx.x), sp = blockIdx.x >> 5, pr = sp & 1, mem = sp >> 1;
  const int wi = AO ? 2 * h + pr : NH * pr + h;
#define PHS (AO == 2 ? (wi * SPAN) >> 6 : (h * SPAN) >> 5)
  const int tid = threadIdx.x, lane = tid & 63, wave = __builtin_amdgcn_readfirstlane(tid >> 6);
  const rsrc_t rw = mkrs(ws, WS_END);
  Tag tag;
  const bool proj = ROF(h) < NPH, ffn = ROF(h) >= NPH && ROF(h) < NPH + 10;
#define ROLES() int bx_ = blockIdx.x; asm volatile("" : "+s"(bx_)); \
  const int ru8 = 8 * (ROF(HOF(bx_)) - (proj ? 0 : NPH)) + (bx_ >> 5), grp = (ru8 * 205) >> 10, cb = ru8 - 5 * grp; (void)cb
  if (__builtin_expect(wave >= 4, 1)) {
    const int w = wave - 4, kk = lane >> 3, sub = lane & 7;
    const size_t wbase = ((size_t)wi * SK + (SK - WIN)) * DH;
    const rsrc_t rk = mkrs(kc + wbase, BSTR + WIN * DH * 2), rv = mkrs(vc + wbase, BSTR + WIN * DH * 2);
    const int voff = ((mem * 8 + w) * 8 + kk) * (DH * 2) + 16 * sub;
    u32x4 tl[NBUF][3];
    u64 tst0;
    asm volatile("s_memrealtime %0" : "=s"(tst0));
    unsigned tstart;
    unsigned late = 0, dstep = 0;
    unsigned xd = 0, sl = 0;
#define PACE(t) do { if ((t) >= PF) { \
      if ((t) >= NBUF && (t) < NBUF + (1 << RSH)) tstart += (((int)dstep * ((t) - NBUF + 1)) >> RSH) - (((int)dstep * ((t) - NBUF)) >> RSH); \
      { unsigned xv_; asm volatile("s_add_u32 %0, %0, %1" : "+s"(tstart) : "s"(sl) : "scc");     \
      asm volatile("1%=:\n\tv_mov_b32 %1, %6\n\tds_read_b32 %1, %1\n\ts_memrealtime s[90:91]\n\ts_waitcnt lgkmcnt(0)\n\tv_readfirstlane_b32 %2, %1\n\ts_sub_u32 %0, s90, %4\n\t" \
                   "s_and_b32 %3, %2, 255\n\ts_lshr_b32 %2, %2, 8\n\ts_addk_i32 %0, %5\n\ts_sub_u32 %0, %0, %2\n\t" \
                   "s_cmp_lt_i32 %0, 0\n\ts_cbranch_scc0 2%=f\n\ts_sleep 1\n\ts_branch 1%=b\n2%=:" \
                   : "=&s"(late), "=&v"(xv_), "=&s"(xd), "=&s"(sl) : "s"(tstart), "n"(-sched(t)), "s"((unsigned)(uintptr_t)&ctl[14]) : "scc", "s90", "s91", "memory"); \
      } \
      if (ckof(t) >= 0) { unsigned oc_;     \
        asm volatile("s_getreg_b32 %0, hwreg(HW_REG_IB_STS, 0, 4)" : "=s"(oc_)); \
        if (ckof(t) >= 0) asm volatile("s_mov_b64 exec, 1\n\tds_add_u32 %0, %1\n\ts_mov_b64 exec, -1" :: "v"((unsigned)(uintptr_t)&ctl[16 + (ckof(t) & 3)]), "v"(PCAP - min(10 * oc_ + late, (unsigned)(ckof(t) == 0 ? CP0 : ckof(t) == 1 ? CP1 : CP2))) : "memory"); \
      } \
      if (PR1) asm volatile("s_setprio 0\n\ts_cmpk_lt_u32 %0, %1\n\ts_cbranch_scc1 1%=f\n\ts_setprio 1\n\ts_cmpk_lt_u32 %0, %2\n\ts_cbranch_scc1 1%=f\n\t" \
                   "s_setprio 2\n\ts_cmpk_lt_u32 %0, %3\n\ts_cbranch_scc1 1%=f\n\ts_setprio 3\n1%=:" \
                   :: "s"(late), "n"(PR1), "n"(PR2), "n"(PR3) : "scc", "memory"); } } while (0)
#define WINDOW(t) do { if (LATE && (t) >= PF && (t) < TSW) \
      asm volatile("s_cmpk_lt_u32 %0, %1\n\ts_cbranch_scc0 1%=f\n\ts_waitcnt vmcnt(%2)\n1%=:\n\ts_waitcnt vmcnt(%3)" \
                   :: "s"(late), "n"(LATE), "n"(WINL - 1), "n"(WINB - 1) : "scc", "memory"); \
      else VMW(((t) < TSW ? WINL : WINV) - 1); } while (0)
#define ISSUE_T(t) do { PACE(t); _Pragma("unroll") for (int i_ = 0; i_ < 3; ++i_) { \
      WINDOW(t); \
      tl[(t) % NBUF][i_] = ldb_nt((t) < NT ? rk : rv, voff + 128 * i_, ((((t) >> 3) & 1) ^ 1) * BSTR + ((t) & 7) * TSTR); \
      SB(); } } while (0)
    SB();
    ISSUE_T(0);
    asm volatile("s_waitcnt lgkmcnt(0)" : "+s"(tst0));
    clk[w] = (unsigned)tst0;
    int tfst = 0;
    if (w == 0) { VMW(2); tfst = (int)((unsigned)__builtin_amdgcn_s_memrealtime() - (unsigned)tst0); }
    BAR();
    tstart = __builtin_amdgcn_readfirstlane(clk[1]) + TOFS + PHS;
    if (w == 0) ctl[14] = (unsigned)(min(max((tfst - L0) * LGN >> 3, 0), RMX) << 8);
#pragma unroll
    for (int t = 1; t < NBUF; ++t) ISSUE_T(t);
    SB();
    LDSWAIT(ctl[13], 1, 2);
    dstep = __builtin_amdgcn_readfirstlane(ctl[12]);
    u32x4 qp[3];
    float sc[NT];
#pragma unroll
    for (int t = 0; t < NT; ++t) {
      SB();
      if ((t & 7) == 0) {
#pragma unroll
        for (int i = 0; i < 3; ++i) qp[i] = *reinterpret_cast<const u32x4*>(&qs[(t >> 3) ^ 1][32 * i + 4 * sub]);
      }
      float d0 = 0.f, d1 = 0.f;
#pragma unroll
      for (int i = 0; i < 3; ++i) {
        d0 = dot2(tl[t % NBUF][i][0], qp[i][0], d0); d1 = dot2(tl[t % NBUF][i][1], qp[i][1], d1);
        d0 = dot2(tl[t % NBUF][i][2], qp[i][2], d0); d1 = dot2(tl[t % NBUF][i][3], qp[i][3], d1);
      }
      asm volatile("" : "+v"(d0), "+v"(d1));
      SB();
      ISSUE_T(t + NBUF);
      SB();
      float d = d0 + d1;
      d += dpp<XOR1>(d); d += dpp<XOR2>(d); d += dpp<XOR7>(d);
      sc[t ^ 8] = rbf(rbf(d) * SCALE);
    }
    float m[2];
#pragma unroll
    for (int bi = 0; bi < 2; ++bi) {
      float mm = sc[8 * bi], l = 0.f;
#pragma unroll
      for (int t = 1; t < 8; ++t) mm = fmaxf(mm, sc[8 * bi + t]);
      mm = fmaxf(mm, dpp<XOR8>(mm)); mm = x16max(mm); mm = x32max(mm);
      m[bi] = mm * LOG2E;
#pragma unroll
      for (int t = 8 * bi; t < 8 * bi + 8; ++t) { sc[t] = __builtin_amdgcn_exp2f(fmaf(sc[t], LOG2E, -m[bi])); l += sc[t]; }
      mlw[0][8 * bi + w] = m[bi]; mlw[1][8 * bi + w] = sum_kk(l);
    }
    LDSINC(ctl[4]);
    u32x4 msk;
#pragma unroll
    for (int r = 0; r < 4; ++r) msk[r] = (sub >> 1) == r ? ((sub & 1) ? 0xffff0000u : 0x0000ffffu) : 0u;
    f32x4 acc[3];
    unsigned mwr = (unsigned)(uintptr_t)&mrg[0][w][32 * ((lane >> 4) & 1) + 4 * sub + 2 * (lane >> 5)];
    unsigned mrd = (unsigned)(uintptr_t)&mrg[1][0][4 * (lane < 48 ? lane : 0)];
    int opub = lane < 48 ? WS_O + (((wi & 31) * NB + (wi >> 5)) * 4 + mem) * (DH * 4) + 16 * ((lane & 48) + 2 * sub + ((lane >> 3) & 1)) : WS_END;
#define DEPOSIT(tick, ...) do { float a_[12]; _Pragma("unroll") for (int i_ = 0; i_ < 12; ++i_) a_[i_] = acc[i_ >> 2][i_ & 3]; \
      asm volatile("s_nop 5\n\tv_permlane32_swap_b32 %0, %2\n\tv_permlane32_swap_b32 %1, %3\n\tv_permlane32_swap_b32 %4, %6\n\tv_permlane32_swap_b32 %5, %7\n\t" \
                   "v_permlane32_swap_b32 %8, %10\n\tv_permlane32_swap_b32 %9, %11\n\t" \
                   "v_add_f32_dpp %0, %2, %0 row_ror:8 row_mask:0xf bank_mask:0xf\n\tv_add_f32_dpp %1, %3, %1 row_ror:8 row_mask:0xf bank_mask:0xf\n\t" \
                   "v_add_f32_dpp %4, %6, %4 row_ror:8 row_mask:0xf bank_mask:0xf\n\tv_add_f32_dpp %5, %7, %5 row_ror:8 row_mask:0xf bank_mask:0xf\n\t" \
                   "v_add_f32_dpp %8, %10, %8 row_ror:8 row_mask:0xf bank_mask:0xf\n\tv_add_f32_dpp %9, %11, %9 row_ror:8 row_mask:0xf bank_mask:0xf\n\t" \
                   "s_mov_b64 exec, %[vm]\n\tds_write2_b32 %[a], %0, %1 offset1:1\n\tds_write2_b32 %[a], %4, %5 offset0:64 offset1:65\n\tds_write2_b32 %[a], %8, %9 offset0:128 offset1:129\n\t" \
                   "s_mov_b64 exec, 1\n\t" tick "\n\ts_mov_b64 exec, -1" \
                   : "+v"(a_[0]), "+v"(a_[1]), "+v"(a_[2]), "+v"(a_[3]), "+v"(a_[4]), "+v"(a_[5]), "+v"(a_[6]), "+v"(a_[7]), "+v"(a_[8]), "+v"(a_[9]), "+v"(a_[10]), "+v"(a_[11]), [o] "=&v"(old) \
                   : [vm] "s"(0x00ff00ff00ff00ffull), [a] "v"(mwr), [one] "v"(1u), __VA_ARGS__ : "memory"); } while (0)
    unsigned old;
#pragma unroll
    for (int i = 0; i < 3; ++i) acc[i] = f32x4{0.f, 0.f, 0.f, 0.f};
#pragma unroll
    for (int t = 0; t < NT; ++t) {
      SB();
      if (t == 7) {
        LDSWAIT(ctl[4], 8, 1);
        tag = mktag(__builtin_amdgcn_readfirstlane(ctl[0]));
#pragma unroll
        for (int bi = 0; bi < 2; ++bi) {
          const f32x4 ma = *reinterpret_cast<const f32x4*>(&mlw[0][8 * bi]), mb = *reinterpret_cast<const f32x4*>(&mlw[0][8 * bi + 4]);
          const float M = rnd22(fmaxf(fmaxf(fmaxf(ma[0], ma[1]), fmaxf(ma[2], ma[3])), fmaxf(fmaxf(mb[0], mb[1]), fmaxf(mb[2], mb[3]))));
          const float wg = __builtin_amdgcn_exp2f(m[bi] - M);
          if (bi == 1) {
#pragma unroll
            for (int i = 0; i < 3; ++i) acc[i] *= wg;
          }
#pragma unroll
          for (int u = bi ? 15 : 0; u < 8 * bi + 8; ++u) sc[u] *= wg;
        }
        asm volatile("" : "+v"(mwr), "+v"(mrd), "+v"(opub), "+s"(tag.t[0]), "+s"(tag.t[1]), "+s"(tag.t[2]), "+s"(tag.t[3]));
        SB();
      }
      const unsigned pd = cvtpk(sc[t ^ 8], sc[t ^ 8]);
      const u32x4 af = {pd & msk[0], pd & msk[1], pd & msk[2], pd & msk[3]};
#pragma unroll
      for (int i = 0; i < 3; ++i) acc[i] = mfma(af, tl[(t + NT) % NBUF][i], acc[i]);
      SB();
      if (t == 7) {
        DEPOSIT("ds_add_u32 %[c], %[one]", [c] "v"((unsigned)(uintptr_t)&ctl[10]));
        mwr += 8 * DH * 4;
#pragma unroll
        for (int i = 0; i < 3; ++i) acc[i] = f32x4{0.f, 0.f, 0.f, 0.f};
      }
      if (viss(t)) ISSUE_T(viss(t));
      if (viss(t) == WTRIG) { unsigned tv_; asm volatile("s_add_u32 %1, %1, %2\n\tv_mov_b32 %0, %1" : "=v"(tv_), "+s"(tstart) : "s"(xd) : "scc"); clk[w] = tv_; LDSINC(ctl[8]); }
      SB();
    }
    {
      f32x4 pv[8];
      DEPOSIT("ds_add_rtn_u32 %[o], %[c], %[one]", [c] "v"((unsigned)(uintptr_t)&ctl[5]));
      typedef __attribute__((address_space(3))) const f32x4* lp4;
      asm volatile("s_waitcnt lgkmcnt(0)" : "+v"(old) :: "memory");
      const unsigned tk = __builtin_amdgcn_readfirstlane(old);
      if (tk == 7u || tk == ET) {
        if (__builtin_expect(tk == ET, 0)) { LDSWAIT(ctl[10], 8, 1); mrd -= 8 * DH * 4; opub += 2 * 4 * DH * 4; }
#pragma unroll
        for (int i = 0; i < 8; ++i) pv[i] = *reinterpret_cast<lp4>(mrd + i * (DH * 4));
        SB();
#pragma unroll
        for (int st = 1; st < 8; st *= 2)
#pragma unroll
          for (int i = 0; i < 8; i += 2 * st) pv[i] += pv[i + st];
        st_ag(rw, opub, g_pack(pv[0][0], pv[0][1], pv[0][2], pv[0][3], tag));
      }
    }
    if (proj || ffn) {
      ROLES();
      const int j = lane & 15, g = lane >> 4;
      typedef __attribute__((address_space(3))) const u32x4* lpu;
      const int wd = (cb * 8 + w) * 64 + lane;
      unsigned ydst = WS_Y + (wd / YP) * YL + (wd % YP) * 8;
      unsigned oo = (g * DM + 128 * cb + 8 * j + w) * 2;
      unsigned ard = (unsigned)(uintptr_t)&oA[j >> 2][4 * g], brd = (unsigned)(uintptr_t)&wf[0][w][lane];
      u32x4 bw[6], af[6];
      f32x4 y = {0.f, 0.f, 0.f, 0.f}, y2 = {0.f, 0.f, 0.f, 0.f};
      auto finish = [&](const float yv) __attribute__((always_inline)) {
        u64 add = __builtin_bit_cast(u64, __builtin_fma((double)yv, 274877906944.0, 6755399441055744.0)) + ((1ull << 57) - 0x4338000000000000ull);
        const bool side = (__float_as_uint(yv) << 1) - (YLO << 24) > ((YHI - YLO) << 24);
        asm volatile("" : "+v"(add));
        if (__builtin_expect(__builtin_amdgcn_ballot_w64(side) != 0, 0)) {
          if (side) {
            __builtin_amdgcn_raw_buffer_store_b64(u32x2{__float_as_uint(yv), ctl[0]}, rw, WS_S + ((proj ? grp : NH + grp) * (NB * DM) + wd) * 8, 0, AUX_SC1);
            __hip_atomic_fetch_or(reinterpret_cast<u64*>(ws + ydst), 1ull << 63, __ATOMIC_RELAXED, __HIP_MEMORY_SCOPE_AGENT);
            add = 1ull << 57;
          }
          VMW(0);
          asm volatile("" : "+v"(ydst));
        }
        u64 tot = __hip_atomic_fetch_add(reinterpret_cast<u64*>(ws + ydst), add, __ATOMIC_RELAXED, __HIP_MEMORY_SCOPE_AGENT);
        SB();
        add += (1ull << 31) - ((u64)NY << 57);
        asm volatile("" : "+v"(add));
        SB();
        tot += add;
        asm volatile("" : "+v"(tot));
        const int hi = (int)(tot >> 31) >> 1;
        if ((unsigned)(hi + (1 << 24)) < (1u << 25)) {
          float z = fmaf((float)hi, 1.f / 64, (float)(int)((unsigned)tot ^ 0x80000000u) * (1.f / 274877906944.f));
          if (__builtin_expect(hi != (int)(tot >> 32), 0)) {
#pragma unroll 1
            for (int a = 0; a < NY; ++a) {
              const u32x2 v = __builtin_amdgcn_raw_buffer_load_b64(rw, WS_S + (a * (NB * DM) + wd) * 8, 0, AUX_SC1);
              if (v[1] == ctl[0]) z += __uint_as_float(v[0]);
            }
          }
          *reinterpret_cast<unsigned short*>(reinterpret_cast<unsigned char*>(out) + oo) = (unsigned short)cvtpk(z, 0.f);
        }
      };
      if (proj && w < 3) {
        const int bb = lane >> 4, dq = 16 * w + (lane & 15);
        const int f0 = WS_O + (grp * NB + bb) * 4 * (DH * 4) + 16 * dq;
        int f[4] = {f0, f0 + DH * 4, f0 + 2 * DH * 4, f0 + 3 * DH * 4};
        unsigned awr = (unsigned)(uintptr_t)&oA[bb][16 * (w + 3 * ((lane >> 3) & 1)) + 2 * (lane & 7)];
        f32x4 r[4]; u32x4 p[12];
        LDSWAIT(ctl[9], 1, 1);
        f32x4 sq = *reinterpret_cast<const f32x4*>(&mlw[2][4 * bb]);
        asm volatile("" : "+v"(sq), "+v"(awr), "+v"(ydst), "+v"(ard), "+v"(brd), "+v"(oo), "+v"(f[0]), "+v"(f[1]), "+v"(f[2]), "+v"(f[3]));
        tstart += sched(31) - PHS + max(PT1 + (((AO ? 32 + grp : 2 * grp) * SPAN) >> 6), WB + PD + ((grp * WPB) >> 5));
        while ((int)((unsigned)__builtin_amdgcn_s_memrealtime() - tstart) < 0) __builtin_amdgcn_s_sleep(1);
        poll4<PSL1, FW1>(r, p, f, rw, tag);
        const f32x4 o = (sq[0] * r[0] + sq[1] * r[1]) + (sq[2] * r[2] + sq[3] * r[3]);
        *reinterpret_cast<__attribute__((address_space(3))) u32x2*>(awr) = u32x2{cvtpk(o[0], o[1]), cvtpk(o[2], o[3])};
        BAR();
#pragma unroll
        for (int ks = 0; ks < 6; ++ks) { af[ks] = *reinterpret_cast<lpu>(ard + 64 * ks); bw[ks] = *reinterpret_cast<lpu>(brd + 8192 * ks); }
        SB();
#pragma unroll
        for (int ks = 0; ks < 6; ks += 2) { y = mfma(af[ks], bw[ks], y); y2 = mfma(af[ks + 1], bw[ks + 1], y2); }
        finish(y[0] + y2[0]);
        poll4_keep(p);
      } else {
        LDSWAIT(ctl[15], 3, 1);
#pragma unroll
        for (int ks = 0; ks < 3; ++ks) bw[ks] = *reinterpret_cast<lpu>(brd + 8192 * ks);
#pragma unroll
        for (int ks = 0; ks < 3; ++ks) asm volatile("" : "+v"(bw[ks]));
        LDSWAIT(ctl[6], 4, 0);
        if (proj) {
#pragma unroll
          for (int ks = 3; ks < 6; ++ks) bw[ks] = *reinterpret_cast<lpu>(brd + 8192 * ks);
#pragma unroll
          for (int ks = 3; ks < 6; ++ks) asm volatile("" : "+v"(bw[ks]));
          asm volatile("" : "+v"(ydst), "+v"(ard), "+v"(oo));
          BAR();
#pragma unroll
          for (int ks = 0; ks < 6; ++ks) af[ks] = *reinterpret_cast<lpu>(ard + 64 * ks);
          SB();
#pragma unroll
          for (int ks = 0; ks < 6; ks += 2) { y = mfma(af[ks], bw[ks], y); y2 = mfma(af[ks + 1], bw[ks + 1], y2); }
        } else {
          y2[0] = bflo(reinterpret_cast<const unsigned short*>(&red[0][0][0])[8 * lane + w]);
#pragma unroll
          for (int ks = 0; ks < 3; ++ks) y = mfma(*reinterpret_cast<lpu>(ard + 64 * ks), bw[ks], y);
        }
        finish(y[0] + y2[0]);
      }
    }
  } else {
    LAND(0);
    const int w = wave;
    unsigned tstart;
    const unsigned town = (unsigned)__builtin_amdgcn_s_memrealtime() + SOFS;
    if (PRS) __builtin_amdgcn_s_setprio(PRS);
    const rsrc_t rc = mkrs(reinterpret_cast<const void*>(pc0 & ~255ull), CODEB);
    const int cofs = 1024 * (blockIdx.x >> 3);
    u32x4 cv = {0u, 0u, 0u, 0u};
    if (CPE && w == 3) cv = ldb(rc, 16 * lane, cofs);
    {
      const int j = lane & 15, g = lane >> 4;
      const bool hq = (h ^ (h >> HQX)) & 1;
      const bool isq = hq || sp >= 4;
      const int fi = sp, cb0 = 3 * (h >> 1);
      const int qu = hq ? sp + 4 : sp - 4, qh = (h & 30) + (qu >= 6), qcol = qh * DH + 64 * ((qu >= 6 ? qu - 6 : qu) >> 1);
      const int ns = isq ? 5 : 8 - (fi & 1), n1 = isq ? 5 : (0x7528 >> (4 * fi)) & 15, s0 = isq ? 5 * (qu & 1) : (15 * fi + 1) >> 1;
      const int ld2 = isq ? NH * DH * 2 : FF * 2;
      const rsrc_t rx = mkrs(x, NB * DM * 2);
      const int vo = (32 * (j >> 3) + 8 * g) * ld2 + 16 * (j & 7), xo = ((j & 3) * DM + 32 * ((j >> 2) & 1) + 8 * g) * 2;
      u32x4 wv[2][8], xa[2], xr[2];
      LAND(1);
      if (ZE && (blockIdx.x >> 3) == 16 && lane < 40) {
        const int wd = 320 * (blockIdx.x & 7) + 80 * w + 2 * lane;
        st_ag(rw, WS_Y + (wd / YP) * YL + (wd % YP) * 8, u32x4{0u, 0u, 0u, 0u});
      }
      const u32x2 ep = __builtin_amdgcn_raw_buffer_load_b64(rw, WS_EPOCH, 0, AUX_SC1);
#pragma unroll
      for (int i = 0; i < 2; ++i) xr[i] = ldb(rx, (w * DM + 16 * (lane < 40 ? lane : 0) + 8 * i) * 2, 0);
#pragma unroll
      for (int st = 0; st < 2; ++st) {
        const int gs = s0 + w + 4 * st, cw = cb0 + (gs >= 10) + (gs >= 20), rs = gs - 10 * ((gs >= 10) + (gs >= 20));
        xa[st] = ldb(rx, xo, 128 * rs);
        if (st == 1) LAND(2);
        if (st == 0 || w + 4 < ns) {
          const bf16* um = isq ? wq + qcol : (cw < 24 ? w1 + 64 * cw : w3 + 64 * (cw - 24));
          const rsrc_t ru = mkrs(um, (unsigned)(DM - 1) * ld2 + 128);
#pragma unroll
          for (int e = 0; e < 8; ++e) { wv[st][e] = ldb_nt(ru, vo, (64 * rs + e) * ld2); SB(); }
        }
      }
      LAND(3);
      if (!ZE && (blockIdx.x >> 3) == 16 && lane < 40) {
        const int wd = 320 * (blockIdx.x & 7) + 80 * w + 2 * lane;
        st_ag(rw, WS_Y + (wd / YP) * YL + (wd % YP) * 8, u32x4{0u, 0u, 0u, 0u});
      }
      if (tid < 18) ctl[2 + tid] = 0u;
      SB();
      VMW(12);
      BAR();
      SB();
      const unsigned tst0 = clk[1];
      const u64 epoch = ((u64)__builtin_amdgcn_readfirstlane(ep[0]) | ((u64)__builtin_amdgcn_readfirstlane(ep[1]) << 32)) + 1ull;
      tag = mktag((unsigned)epoch);
      u32x4 stg = s_tag((unsigned)epoch, lane);
      const int da = isq ? WS_Q + ((qu & 1) * (NH * DH) + qcol) * 8 : WS_GU + ((fi > 0 ? 2 * FF : 0) + 64 * (cb0 + (fi > 0 ? fi - 1 : 0))) * 8;
      int dst = (lane < 32 ? da : n1 < ns ? WS_GU + 64 * (cb0 + fi) * 8 : WS_END) + 16 * (lane & 31);
      unsigned rdr = (unsigned)(uintptr_t)&red[lane >> 5][2 * (lane & 3)][(lane & 31) >> 2];
      asm volatile("" : "+v"(stg), "+v"(dst), "+v"(rdr), "+s"(tag.t[0]), "+s"(tag.t[1]), "+s"(tag.t[2]), "+s"(tag.t[3]));
      {
        float v[16]; float sm = 0.f;
#pragma unroll
        for (int i = 0; i < 2; ++i)
#pragma unroll
          for (int k = 0; k < 4; ++k) { v[8 * i + 2 * k] = bflo(xr[i][k]); v[8 * i + 2 * k + 1] = bfhi(xr[i][k]); sm += v[8 * i + 2 * k] + v[8 * i + 2 * k + 1]; }
        const float mean = wave_sum(lane < 40 ? sm : 0.f) * (1.f / DM);
        float sq = 0.f;
#pragma unroll
        for (int k = 0; k < 16; ++k) { v[k] -= mean; sq += v[k] * v[k]; }
        const float var = wave_sum(lane < 40 ? sq : 0.f) * (1.f / DM);
        stat[w][0] = rbf(mean); stat[w][1] = 1.f / rbf(sqrtf(rbf(rbf(var) + 1e-5f)));
        LDSINC(ctl[7]);
        LDSWAIT(ctl[7], 4, 0);
      }
      SB();
      const float mean = stat[j & 3][0], rinv = stat[j & 3][1];
#pragma unroll
      for (int st = 0; st < 2; ++st) {
        if (st == 0 || w + 4 < ns) {
          u32x4 af;
#pragma unroll
          for (int p = 0; p < 4; ++p) {
            const unsigned c = cvtpk(bflo(xa[st][p]) - mean, bfhi(xa[st][p]) - mean);
            af[p] = cvtpk(bflo(c) * rinv, bfhi(c) * rinv);
          }
          f32x4 d[8];
#pragma unroll
          for (int ec = 0; ec < 8; ++ec) d[ec] = mfma(af, bfrag(&wv[st][0], ec), f32x4{0.f, 0.f, 0.f, 0.f});
          if (g == (j >> 3)) {
#pragma unroll
            for (int ec = 0; ec < 8; ++ec) red[2 * (w + 4 * st) + g][ec][j & 7] = d[ec];
          }
        }
      }
      tstart = __builtin_amdgcn_readfirstlane(tst0);
      asm volatile("" :: "v"(cv));
      if (tid == 0) *reinterpret_cast<u64*>(&ctl[0]) = epoch;
      asm volatile("s_waitcnt vmcnt(0) lgkmcnt(0)" ::: "memory");
      unsigned old = 0;
      if (lane == 0) old = __hip_atomic_fetch_add(&ctl[2], 1u, __ATOMIC_RELAXED, __HIP_MEMORY_SCOPE_WORKGROUP);
      const bool last = __builtin_amdgcn_readfirstlane(old) == 3u;
      FENCE();
      if (__builtin_amdgcn_readfirstlane(old) == 0u) {
        typedef const unsigned char* cp;
        ROLES();
        const size_t wb = ((size_t)wi * SK + (SK - WIN)) * DH * 2 + (j & 8) * 49152 + (j & 1) * 393088;
        const cp tp = g == 2 && TOL >= 0 ? ws + WS_O + (((wi & 31) * NB + (wi >> 5) + (j < 6 ? 0 : 2)) * 4 + mem) * (DH * 4) + TOL * (j < 6 ? j : j < 12 ? j - 6 : 0)
                    : g == 3 ? (j < 15 || !TCK ? ws + WS_Y + ((j & TYM) * (WS_S - WS_Y) / 15 & ~255) + 192 : ws + WS_CK + (TCK == 2 ? (blockIdx.x >> 4 & 3) * 8192 + (blockIdx.x & 15) * 128 : blockIdx.x * 128))
                    : mem == 0 ? (cp)kc + wb : mem == 1 ? (cp)vc + wb : mem == 2 ? (cp)vc + wb + BSTR
                    : j < 8 && proj ? (cp)wo + min(DH * (((ru8 & ~7) * 205) >> 10) + 36 * ((2 * j + pr) & TWM), NH * DH - 1) * (DM * 2)
                    : j < 8 && ffn ? (cp)w2 + min(96 * (((ru8 & ~7) * 205) >> 10) + 18 * ((2 * j + pr) & TWM), FF - 1) * (DM * 2)
                    : ws + WS_O + min(j & TOM, 5) * 65536 + 65472;
        unsigned tv;
        asm volatile("global_load_dword %0, %1, off sc1\n\ts_waitcnt vmcnt(0)" : "=&v"(tv) : "v"(tp) : "memory");
      }
      if (last) {
        {
          typedef __attribute__((address_space(3))) const f32x4* lp4;
          u32x4 gs;
          f32x4 v[8][2], va[2], vb[2] = {};
#pragma unroll
          for (int p = 0; p < 5; ++p) { v[p][0] = *reinterpret_cast<lp4>(rdr + 2304 * p); v[p][1] = *reinterpret_cast<lp4>(rdr + 2304 * p + 144); }
          if (isq) {
            SB();
#pragma unroll
            for (int k = 0; k < 2; ++k) va[k] = ((v[0][k] + v[1][k]) + (v[2][k] + v[3][k])) + v[4][k];
          } else {
#pragma unroll
            for (int p = 5; p < 8; ++p) { const unsigned a = rdr + 2304 * (p == 7 ? ns - 1 : p); v[p][0] = *reinterpret_cast<lp4>(a); v[p][1] = *reinterpret_cast<lp4>(a + 144); }
            SB();
#pragma unroll
            for (int k = 0; k < 2; ++k) {
              va[k] = v[0][k] + v[1][k];
#pragma unroll
              for (int p = 2; p < 8; ++p) { va[k] += (p < n1 ? 1.f : 0.f) * v[p][k]; vb[k] += (p >= n1 && p < ns ? 1.f : 0.f) * v[p][k]; }
            }
          }
#pragma unroll
          for (int k = 0; k < 2; ++k) {
            const u32x2 pk = s_pack(f32x4{tr32(va[k][0], vb[k][0]), tr32(va[k][1], vb[k][1]), tr32(va[k][2], vb[k][2]), tr32(va[k][3], vb[k][3])}, stg[2 * k], stg[2 * k + 1]);
            gs[2 * k] = pk[0]; gs[2 * k + 1] = pk[1];
          }
          st_ag(rw, dst, gs);
        }
        if ((blockIdx.x >> 3) == 16 && lane < TREP) st_ag(rw, WS_TM + 128 * lane + 16 * (blockIdx.x & 7), g_int(tstart, ((unsigned)__builtin_amdgcn_s_memrealtime() - tstart) & 4095u, tag));
        while ((unsigned)__builtin_amdgcn_s_memrealtime() - town < (unsigned)QT) __builtin_amdgcn_s_sleep(4);
        const int qo = WS_Q + (wi & 31) * DH * 8 + 16 * (lane < 48 ? lane : lane & 3);
        const int qo0 = lane >= 48 ? WS_TM + 128 * (h & (TREP - 1)) + 16 * (lane & 7) : qo;
        const u32x4 tg0 = lane < 48 ? stg : u32x4{tag.t[0], tag.t[1], tag.t[2], tag.t[3]};
        u32x4 g0, g1, g2, g3;
#pragma unroll 1
        for (int it = 0; it < SPIN_MAX; ++it) {
          FENCE();
          g0 = ld_ag(rw, qo0); g1 = ld_ag(rw, qo + NH * DH * 8);
          g2 = ld_ag(rw, qo + 768); g3 = ld_ag(rw, qo + NH * DH * 8 + 768);
          if (__all(((s_dif(g0, tg0) & (lane < 48 ? 15u : 1023u)) | ((s_dif(g1, stg) | s_dif(g2, stg) | s_dif(g3, stg)) & 15u)) == 0u)) break;
          __builtin_amdgcn_s_sleep(QBACK);
        }
        {
          const float d = fminf(fmaxf((float)((int)((g0[0] & ~1023u) - (tstart << 10)) >> 10), -300.f), 300.f);
          float sm = d + dpp<XOR1>(d); sm += dpp<XOR2>(sm); sm += dpp<XOR7>(sm);
          float mx = fminf(d, dpp<XOR1>(d)); mx = fminf(mx, dpp<XOR2>(mx)); mx = fminf(mx, dpp<XOR7>(mx));
          float lx = fmaxf(d, dpp<XOR1>(d)); lx = fmaxf(lx, dpp<XOR2>(lx)); lx = fmaxf(lx, dpp<XOR7>(lx));
          const float db = rintf(fmaxf(sm * (0.125f - CAL / 64.f) + mx * (CAL / 8.f), lx - CUP));
          if (lane == 48) { ctl[12] = (unsigned)(int)db; ctl[11] = (unsigned)(int)(lx - db); }
          float rd = (float)(g0[2] >> 10); rd += dpp<XOR1>(rd); rd += dpp<XOR2>(rd); rd += dpp<XOR7>(rd);
          if (lane == 48) ctl[14] = ((unsigned)fminf(fmaxf((rd * 0.125f - R0) * (RG / 8.f), 0.f), RMX) << 8);
        }
#pragma unroll
        for (int r = 0; r < 2; ++r) {
          const u32x4 ga = r ? g2 : g0, gb = r ? g3 : g1;
          float q[2][2];
#pragma unroll
          for (int k = 0; k < 2; ++k) {
            const float sa = s_scale(ga[2 * k], ga[2 * k + 1]), sb = s_scale(gb[2 * k], gb[2 * k + 1]);
            const unsigned a = wi < NH ? ga[2 * k] : ga[2 * k + 1], b = wi < NH ? gb[2 * k] : gb[2 * k + 1];
            q[0][k] = fmaf(s_hi(a), sa, s_hi(b) * sb); q[1][k] = fmaf(s_lo(a), sa, s_lo(b) * sb);
          }
          if (lane < 48) { qs[0][48 * r + lane] = cvtpk(q[0][0], q[0][1]); qs[1][48 * r + lane] = cvtpk(q[1][0], q[1][1]); }
        }
      }
      if (last) LDSINC(ctl[13]);
      LDSWAIT(ctl[13], 1, 8);
      tstart += __builtin_amdgcn_readfirstlane(ctl[12]);
    }
    if (NCK && w == 3) {
      unsigned xt = __builtin_amdgcn_readfirstlane(ctl[14]) >> 8, sl = 0;
      float pp = 0.f;
      const int ds = __builtin_amdgcn_readfirstlane(ctl[12]), dl = __builtin_amdgcn_readfirstlane(ctl[11]);
#pragma unroll 1
      for (int k = 0; k < NCK; ++k) {
        const int tl = ckt(k) + 1;
        const unsigned tp = tstart + TOFS + SPAN + CKO + sched(tl) + xt + 2 * sl;
        const int rem = min(max(NBUF - 1 + (1 << RSH) - tl, 0), 1 << RSH);
        while ((int)((unsigned)__builtin_amdgcn_s_memrealtime() - tp) < -((ds * rem) >> RSH)) __builtin_amdgcn_s_sleep(8);
        FENCE();
        const unsigned cr = ctl[16 + k];
        if ((h >> 3) == (sp & 3) && ((sp ^ (h >> 1)) & RPM) == 0 && lane < NREP) st_ag(rw, WS_CK + k * 8192 + lane * 1024 + (RPM == 0 ? sp * 8 + (h & 7) : RPM == 4 ? (sp & 3) * 8 + (h & 7) : (sp & 1) * 8 + (h & 7)) * 16, g_int(cr, 0u, tag));
        while ((int)((unsigned)__builtin_amdgcn_s_memrealtime() - tp) < CRD + ((dl * rem) >> RSH)) __builtin_amdgcn_s_sleep(4);
        u32x4 g0;
#pragma unroll 1
        for (int it = 0; it < 4; ++it) {
          FENCE();
          g0 = ld_ag(rw, WS_CK + k * 8192 + (h & (NREP - 1)) * 1024 + (lane & (RPM == 0 ? 63 : RPM == 4 ? 31 : 15)) * 16);
          if (__all(g_ok(g0, tag))) break;
        }
        const float p = PCAP - wave_sum(g_ok(g0, tag) ? (float)min(g0[0] >> 10, 4096u) : 0.f) * (1.f / 1024);
        const float df = k == 0 ? 0.1f * CG0 * (p - CZ0) : k < NCK - 1 ? 0.1f * CG1 * (p - CZ1)
                                 : fminf(fmaxf(0.1f * MA * (p - MZ), 0.1f * MB * (p - MY)), MC + 0.1f * MD * p);
        const unsigned d = (unsigned)fmaxf(fminf(df, 250.f), 0.f);
        xt = __builtin_amdgcn_readfirstlane(xt + d);
        if (k == 1) sl = __builtin_amdgcn_readfirstlane(p > SLZ && p > pp - SLD ? SLM : 0);
        if (k == 2 && p < SLY) sl = 0;
        pp = p;
        if (lane == 0) ctl[14] = (xt << 8) | sl;
      }
    }
    if (CPL && w == 2) {
      LDSWAIT(ctl[4], 1, 16);
      cv = ldb(rc, 16 * lane, cofs);
      asm volatile("" :: "v"(cv));
    }
    if (w == 1) {
      LDSWAIT(ctl[4], 8, 16);
      const float mi = mlw[0][lane & 15], li = mlw[1][lane & 15];
      float M = fmaxf(mi, dpp<XOR1>(mi)); M = fmaxf(M, dpp<XOR2>(M)); M = rnd22(fmaxf(M, dpp<XOR7>(M)));
      float L = li * __builtin_amdgcn_exp2f(mi - M);
      L += dpp<XOR1>(L); L += dpp<XOR2>(L); L += dpp<XOR7>(L);
      const u32x4 gp = g_pack(M, L, dpp<XOR8>(M), dpp<XOR8>(L), tag);
      if (lane == 0) st_ag(rw, WS_ML + (wi & 31) * MLH + (2 * mem + (wi >> 5)) * MLS, gp);
      if (blockIdx.x == 255) {
        const unsigned xw = __builtin_amdgcn_readfirstlane(ctl[14]), tml = tstart + (xw >> 8) + 10 * (xw & 255u);
        while ((unsigned)__builtin_amdgcn_s_memrealtime() - tml < (unsigned)sched(24) + TOFS + MLD + 30) __builtin_amdgcn_s_sleep(8);
        bool ok = false;
#pragma unroll 1
        for (int it = 0; it < SPIN_MAX && !ok; ++it) {
          FENCE();
          ok = true;
#pragma unroll
          for (int i = 0; i < 4; ++i) ok &= g_ok(ld_ag(rw, WS_ML + (8 * i + (lane >> 3)) * MLH + (lane & 7) * MLS), tag);
          ok = __all(ok);
          if (!ok) __builtin_amdgcn_s_sleep(32);
        }
        if (ok && lane == 0) __hip_atomic_store(reinterpret_cast<u64*>(ws + WS_EPOCH), *reinterpret_cast<const u64*>(&ctl[0]), __ATOMIC_RELAXED, __HIP_MEMORY_SCOPE_AGENT);
      }
      if (proj) {
        ROLES();
        const unsigned xw = __builtin_amdgcn_readfirstlane(ctl[14]), tml = tstart + (xw >> 8) + 10 * (xw & 255u);
        while ((unsigned)__builtin_amdgcn_s_memrealtime() - tml < (unsigned)sched(24) + TOFS + MLD + 30) __builtin_amdgcn_s_sleep(8);
        u32x4 g0;
#pragma unroll 1
        for (int it = 0; it < SPIN_MAX; ++it) {
          FENCE();
          g0 = ld_ag(rw, WS_ML + grp * MLH + (2 * (lane & 3) + ((lane >> 2) & 1)) * MLS);
          if (__all(g_ok(g0, tag))) break;
          __builtin_amdgcn_s_sleep(12);
        }
        const f32x4 a0 = g_val(g0, tag);
        const float mq = (lane & 8) ? a0[2] : a0[0], lq = (lane & 8) ? a0[3] : a0[1];
        float Mx = fmaxf(mq, dpp<XOR1>(mq)); Mx = fmaxf(Mx, dpp<XOR2>(Mx));
        const float e0 = __builtin_amdgcn_exp2f(mq - Mx);
        float lt = e0 * lq; lt += dpp<XOR1>(lt); lt += dpp<XOR2>(lt);
        mlw[2][lane & 15] = e0 / lt;
        LDSINC(ctl[9]);
      }
    }
    ROLES();
    if (w == 0 && ffn) {
      while ((int)((unsigned)__builtin_amdgcn_s_memrealtime() - tstart) < FGT + PHS) __builtin_amdgcn_s_sleep(8);
      const int go = WS_GU + 96 * grp * 8 + 16 * (lane < 48 ? lane : lane & 3);
      const u32x4 stg = s_tag(__builtin_amdgcn_readfirstlane(ctl[0]), lane);
      u32x4 g[4];
      const u32x4 xv = ldb(mkrs(x, grp == 0 ? NB * DM * 2 : 0), ((lane >> 4) * DM + 128 * cb + 8 * (lane & 15)) * 2, 0);
#pragma unroll 1
      for (int it = 0; it < SPIN_MAX; ++it) {
        FENCE();
#pragma unroll
        for (int i = 0; i < 4; ++i) g[i] = ld_ag(rw, go + i * FF * 8);
        if (__all(((s_dif(g[0], stg) | s_dif(g[1], stg) | s_dif(g[2], stg) | s_dif(g[3], stg)) & 15u) == 0u)) break;
        __builtin_amdgcn_s_sleep(32);
      }
      float hv[2][NB];
#pragma unroll
      for (int k = 0; k < 2; ++k) {
        float sc4[4];
#pragma unroll
        for (int i = 0; i < 4; ++i) sc4[i] = s_scale(g[i][2 * k], g[i][2 * k + 1]);
#pragma unroll
        for (int bb = 0; bb < NB; ++bb) {
          float v[4];
#pragma unroll
          for (int i = 0; i < 4; ++i) v[i] = bb < 2 ? s_hi(g[i][2 * k + bb]) : s_lo(g[i][2 * k + bb - 2]);
          const float gg = rbf(fmaf(v[0], sc4[0], v[2] * sc4[2])), up = rbf(fmaf(v[1], sc4[1], v[3] * sc4[3]));
          hv[k][bb] = rbf(gg / (1.f + __expf(-gg))) * up;
        }
      }
#pragma unroll
      for (int bb = 0; bb < NB; ++bb) oA[bb][lane] = cvtpk(hv[0][bb], hv[1][bb]);
      reinterpret_cast<u32x4*>(&red[0][0][0])[lane] = xv;
    }
    if (w < 3 && (proj || ffn)) {
      LDSWAIT(ctl[8], 8, 2);
      tstart = __builtin_amdgcn_readfirstlane(clk[1]) + sched(31) - PHS;
      while ((int)((unsigned)__builtin_amdgcn_s_memrealtime() - tstart) < (proj ? WT + ((grp * WPH) >> 5) : WTF + ((grp * WPF) >> 4))) __builtin_amdgcn_s_sleep(1);
      const rsrc_t ru = mkrs(proj ? wo + (size_t)(grp * DH + 64 * w) * DM : w2 + (size_t)(96 * grp + 32 * w) * DM, (proj ? 64 : 32) * DM * 2);
      const int vo = 8 * (lane >> 4) * (DM * 2) + 256 * cb + 16 * (lane & 15);
      u32x4 wl[16];
#pragma unroll
      for (int e = 0; e < 8; ++e) wl[e] = ldb_nt(ru, vo, e * (DM * 2));
      SB();
      if (proj) {
        while ((int)((unsigned)__builtin_amdgcn_s_memrealtime() - tstart) < WB + ((grp * WPB) >> 5)) __builtin_amdgcn_s_sleep(1);
        SB();
#pragma unroll
        for (int e = 0; e < 8; ++e) wl[8 + e] = ldb_nt(ru, vo, (32 + e) * (DM * 2));
        SB();
#pragma unroll
        for (int ec = 0; ec < 8; ++ec) wf[w][ec][lane] = bfrag(&wl[0], ec);
        LDSINC(ctl[15]);
        SB();
        u32x4 bw[8];
#pragma unroll
        for (int p = 0; p < 3; ++p)
#pragma unroll
          for (int ec = 0; ec < 8; ++ec) { bw[ec][p] = bfrag(&wl[8], ec)[p]; asm volatile("" : "+v"(bw[ec][p])); }
        SB();
#pragma unroll
        for (int ec = 0; ec < 8; ++ec) { bw[ec][3] = bfrag(&wl[8], ec)[3]; wf[3 + w][ec][lane] = bw[ec]; }
      } else {
#pragma unroll
        for (int ec = 0; ec < 8; ++ec) wf[w][ec][lane] = bfrag(&wl[0], ec);
        LDSINC(ctl[15]);
      }
    }
    LDSINC(ctl[6]);
    if (proj && w < 3) BAR();
  }
}

extern "C" void kernel_launch(void* const* d_in, const int* in_sizes, int n_in, void* d_out, int out_size, void* d_ws,
                              size_t ws_size, hipStream_t stream) {
  fused_k<<<256, 768, 0, stream>>>((const bf16*)d_in[0], (const bf16*)d_in[1], (const bf16*)d_in[2], (const bf16*)d_in[3],
                                   (const bf16*)d_in[4], (const bf16*)d_in[6], (const bf16*)d_in[7], (const bf16*)d_in[8],
                                   (unsigned char*)d_ws, (bf16*)d_out);
}
```

```cpp
#include <hip/hip_runtime.h>
#include <hip/hip_bf16.h>
#include <cstdint>

using bf16 = __hip_bfloat16;
using u32x2 = __attribute__((ext_vector_type(2))) unsigned;
using u32x4 = __attribute__((ext_vector_type(4))) unsigned;
using f32x2 = __attribute__((ext_vector_type(2))) float;
using f32x4 = __attribute__((ext_vector_type(4))) float;
using bf16x2 = __attribute__((ext_vector_type(2))) __bf16;
using bf16x8 = __attribute__((ext_vector_type(8))) __bf16;
typedef unsigned long long u64;

constexpr int NB = 4, DM = 640, NH = 32, DH = 192, SK = 8192, WIN = 2048, FF = 1536;
constexpr float LOG2E = 1.4426950408889634f;
constexpr float SCALE = 0.07216878364870322f;
constexpr int WINL = 4;
constexpr int WINV = 12;
constexpr int TSW = 24;
constexpr int QT = 570;
constexpr int QBACK = 16;
constexpr int TAU = 86;
constexpr int TK = 11;
constexpr int TAU2 = 160;
constexpr int PR1 = 60;
constexpr int PR2 = 150;
constexpr int PR3 = 300;
constexpr int PRS = 3;
constexpr int CPE = 1;
constexpr int CPL = 1;
constexpr int CODEB = 29440;
constexpr int PT1 = 195;
constexpr int ET = 5;
constexpr int SPAN = 80;
constexpr int TOFS = 116;
constexpr int RING = 9;
constexpr int WTRIG = 31;
constexpr int LATE = 100;
constexpr int WINB = 8;
constexpr int MLD = 300;
constexpr int PSL1 = 6;
constexpr int RSH = 3;
constexpr int CAL = 5;
constexpr int LG = 4;
constexpr int WT = 78;
constexpr int FW1 = 1;
constexpr int WTF = 122;
constexpr int NCK = 3;
constexpr int CK0 = 7;
constexpr int CKL = 18;
constexpr int CKS = 6;
constexpr int CG0 = 36;
constexpr int CZ0 = 30;
constexpr int CG1 = 30;
constexpr int CZ1 = 20;
constexpr int MA = 22;
constexpr int MZ = 18;
constexpr int MB = 200;
constexpr int MY = 27;
constexpr int MC = 60;
constexpr int MD = 10;
constexpr int CRD = 110;
constexpr int YP = 8;
constexpr int YL = 256;
constexpr int YLO = 99u;
constexpr int YHI = 139u;
constexpr int WB = 144;
constexpr int WPH = 44;
constexpr int WPB = 50;
constexpr int PD = 52;
constexpr int CKO = 35;
constexpr int RG = 8;
constexpr int R0 = 415;
constexpr int RMX = 120;
constexpr int LGN = 12;
constexpr int L0 = 75;
constexpr int NREP = 2;
constexpr int MLS = 16;
constexpr int MLH = 256;
constexpr int FGT = 1010;
constexpr int TREP = 2;
constexpr int TCK = 1;
constexpr int TYM = 15;
constexpr int TWM = 15;
constexpr int TOM = 7;
constexpr int RPM = 4;
constexpr int CP0 = 100;
constexpr int CP1 = 60;
constexpr int CP2 = 45;
constexpr int CUP = 65;
constexpr int SLZ = 29;
constexpr int SLD = 5;
constexpr int SLM = 4;
constexpr int SLY = 18;
constexpr int TGT = 28;
constexpr int TGG = 10;
constexpr int NPH = 20;
constexpr int WPF = 22;
constexpr int SOFS = 30;
constexpr int UID = 0;
constexpr int PF = 4;
constexpr int ALT = 0;
constexpr int AO = 0;
constexpr int ZE = 0;
constexpr int TOL = 0;
constexpr int RB = 0;
constexpr int HQX = 5;
constexpr int sched(int t) { return t <= TK ? TAU * t : TAU * TK + ((TAU2 * (t - TK)) >> 1) + (t >= TGT ? TGG : 0); }
constexpr int viss0(int v) { return v <= 2 * (RING - 1 - LG) + 1 ? ((v & 1) ? 16 + RING + (v >> 1) : 0) : 16 + v + LG; }
constexpr int viss(int v) { return viss0(v) < 32 ? viss0(v) : 0; }
constexpr int PCAP = 150;
constexpr int ckt(int k) { return k == NCK - 1 ? CKL : CK0 + k * CKS; }
constexpr int ckof(int t) { for (int k = 0; k < NCK; ++k) if (t == ckt(k) || t == ckt(k) + 1) return k; return -1; }
constexpr int NBUF = RING;
constexpr int NT = 16;
constexpr int TSTR = 4 * 8 * 8 * DH * 2;
constexpr int BSTR = 2 * NH * SK * DH * 2;

constexpr int WS_EPOCH = 0;
constexpr int WS_Q = 512;
constexpr int WS_GU = WS_Q + 2 * NH * DH * 8;
constexpr int WS_ML = WS_GU + 2 * 2 * FF * 8;
constexpr int WS_TM = WS_ML + NH * MLH;
constexpr int WS_CK = WS_TM + 8 * 128;
constexpr int WS_O = WS_CK + 4 * 8 * 1024;
constexpr int NY = NH + 16;
constexpr int WS_Y = (WS_O + NH * NB * 4 * DH * 4 + 65535) & ~65535;
constexpr int WS_S = WS_Y + (NB * DM / YP) * YL;
constexpr int WS_END = WS_S + NY * NB * DM * 8;

__device__ __forceinline__ float dot2(unsigned k, unsigned q, float acc) {
  return __builtin_amdgcn_fdot2_f32_bf16(__builtin_bit_cast(bf16x2, k), __builtin_bit_cast(bf16x2, q), acc, false);
}
__device__ __forceinline__ float bflo(unsigned v) { return __builtin_bit_cast(float, v << 16); }
__device__ __forceinline__ float bfhi(unsigned v) { return __builtin_bit_cast(float, v & 0xffff0000u); }
template <int CTRL> __device__ __forceinline__ float dpp(float x) {
  return __builtin_bit_cast(float, __builtin_amdgcn_mov_dpp(__builtin_bit_cast(int, x), CTRL, 0xf, 0xf, true));
}
constexpr int XOR1 = 0xB1, XOR2 = 0x4E, XOR7 = 0x141, XOR8 = 0x128;
__device__ __forceinline__ float x16sum(float x) {
  auto s = __builtin_amdgcn_permlane16_swap(__float_as_uint(x), __float_as_uint(x), false, false);
  return __uint_as_float(s[0]) + __uint_as_float(s[1]);
}
__device__ __forceinline__ float x32sum(float x) {
  auto t = __builtin_amdgcn_permlane32_swap(__float_as_uint(x), __float_as_uint(x), false, false);
  return __uint_as_float(t[0]) + __uint_as_float(t[1]);
}
__device__ __forceinline__ float x16max(float x) {
  auto s = __builtin_amdgcn_permlane16_swap(__float_as_uint(x), __float_as_uint(x), false, false);
  return fmaxf(__uint_as_float(s[0]), __uint_as_float(s[1]));
}
__device__ __forceinline__ float x32max(float x) {
  auto t = __builtin_amdgcn_permlane32_swap(__float_as_uint(x), __float_as_uint(x), false, false);
  return fmaxf(__uint_as_float(t[0]), __uint_as_float(t[1]));
}
__device__ __forceinline__ float sum_kk(float x) { x += dpp<XOR8>(x); x = x16sum(x); return x32sum(x); }
__device__ __forceinline__ float wave_sum(float x) {
  x += dpp<XOR1>(x); x += dpp<XOR2>(x); x += dpp<XOR7>(x); return sum_kk(x);
}
__device__ __forceinline__ float tr32(float a, float b) {
  auto t = __builtin_amdgcn_permlane32_swap(__float_as_uint(a), __float_as_uint(b), false, false);
  return __uint_as_float(t[0]) + __uint_as_float(t[1]);
}
__device__ __forceinline__ float tr16(float a, float b) {
  auto t = __builtin_amdgcn_permlane16_swap(__float_as_uint(a), __float_as_uint(b), false, false);
  return __uint_as_float(t[0]) + __uint_as_float(t[1]);
}
__device__ __forceinline__ unsigned cvtpk(float lo, float hi) {
  return __builtin_bit_cast(unsigned, __builtin_convertvector(f32x2{lo, hi}, bf16x2));
}
__device__ __forceinline__ float rbf(float v) { return bflo(cvtpk(v, 0.f)); }
__device__ __forceinline__ f32x4 mfma(u32x4 a, u32x4 b, f32x4 c) {
  return __builtin_amdgcn_mfma_f32_16x16x32_bf16(__builtin_bit_cast(bf16x8, a), __builtin_bit_cast(bf16x8, b), c, 0, 0, 0);
}
__device__ __forceinline__ u32x4 bfrag(const u32x4* r, int ec) {
  u32x4 f;
#pragma unroll
  for (int p = 0; p < 4; ++p) f[p] = __builtin_amdgcn_perm(r[2 * p + 1][ec >> 1], r[2 * p][ec >> 1], (ec & 1) ? 0x07060302u : 0x05040100u);
  return f;
}
typedef __amdgpu_buffer_rsrc_t rsrc_t;
__device__ __forceinline__ rsrc_t mkrs(const void* p, unsigned n) { return __builtin_amdgcn_make_buffer_rsrc(const_cast<void*>(p), (short)0, (int)n, 0x00020000); }
constexpr int AUX_NT = 2, AUX_SC1 = 16;
__device__ __forceinline__ u32x4 ldb(rsrc_t rs, int voff, int soff) { return __builtin_amdgcn_raw_buffer_load_b128(rs, voff, soff, 0); }
__device__ __forceinline__ u32x4 ldb_nt(rsrc_t rs, int voff, int soff) { return __builtin_amdgcn_raw_buffer_load_b128(rs, voff, soff, AUX_NT); }
__device__ __forceinline__ u32x4 ld_ag(rsrc_t rs, int voff) { return __builtin_amdgcn_raw_buffer_load_b128(rs, voff, 0, AUX_SC1); }
__device__ __forceinline__ void st_ag(rsrc_t rs, int voff, u32x4 v) { __builtin_amdgcn_raw_buffer_store_b128(v, rs, voff, 0, AUX_SC1); }
struct Tag { unsigned t[4]; };
__device__ __forceinline__ Tag mktag(unsigned e) { return Tag{{e & 1023u, (e + 2 * (e >> 10)) & 1023u, (e + 0x156u) & 1023u, (e + 2 * (e >> 19) + 0x2a8u) & 1023u}}; }
__device__ __forceinline__ unsigned r22(float v) { return (__float_as_uint(v) + 0x200u) & ~1023u; }
__device__ __forceinline__ float rnd22(float v) { return __uint_as_float(r22(v)); }
__device__ __forceinline__ u32x4 g_pack(float a, float b, float c, float d, Tag t) {
  return u32x4{r22(a) | t.t[0], r22(b) | t.t[1], r22(c) | t.t[2], r22(d) | t.t[3]};
}
__device__ __forceinline__ u32x4 g_int(unsigned a, unsigned c, Tag t) { return u32x4{(a << 10) | t.t[0], t.t[1], (c << 10) | t.t[2], t.t[3]}; }
__device__ __forceinline__ bool g_ok(u32x4 g, Tag t) { return (((g[0] ^ t.t[0]) | (g[1] ^ t.t[1]) | (g[2] ^ t.t[2]) | (g[3] ^ t.t[3])) & 1023u) == 0u; }
__device__ __forceinline__ f32x4 g_val(u32x4 g, Tag t) {
  return f32x4{__uint_as_float(g[0] ^ t.t[0]), __uint_as_float(g[1] ^ t.t[1]), __uint_as_float(g[2] ^ t.t[2]), __uint_as_float(g[3] ^ t.t[3])};
}
__device__ __forceinline__ u32x4 s_tag(unsigned e, int lane) {
  u32x4 t;
#pragma unroll
  for (int i = 0; i < 4; ++i) t[i] = (e + 2 * (e >> (4 + 7 * i + 2 * (lane & 3)))) & 15u;
  return t;
}
__device__ __forceinline__ u32x2 s_pack(f32x4 v, unsigned t0, unsigned t1) {
  const unsigned eb = max(max(__float_as_uint(v[0]) << 1, __float_as_uint(v[1]) << 1), max(__float_as_uint(v[2]) << 1, __float_as_uint(v[3]) << 1)) >> 24;
  unsigned n[4];
#pragma unroll
  for (int i = 0; i < 4; ++i) n[i] = (unsigned)min((int)rintf(__builtin_amdgcn_ldexpf(v[i], 137 - (int)eb)), 2047);
  return u32x2{(n[0] << 20) | ((n[2] & 4095u) << 8) | ((eb & 15u) << 4) | t0, (n[1] << 20) | ((n[3] & 4095u) << 8) | (eb & 240u) | t1};
}
__device__ __forceinline__ unsigned s_dif(u32x4 g, u32x4 t) { return (g[0] ^ t[0]) | (g[1] ^ t[1]) | (g[2] ^ t[2]) | (g[3] ^ t[3]); }
__device__ __forceinline__ float s_scale(unsigned a, unsigned b) { return __uint_as_float(((a & 240u) << 19) | ((b & 240u) << 23)) * (1.f / 1024); }
__device__ __forceinline__ float s_hi(unsigned a) { return (float)((int)a >> 20); }
__device__ __forceinline__ float s_lo(unsigned a) { return (float)((int)(a << 12) >> 20); }
constexpr int SPIN_MAX = 1 << 18;
#define SB() __builtin_amdgcn_sched_barrier(0)
#define FENCE() asm volatile("" ::: "memory")
#define BAR() asm volatile("s_waitcnt lgkmcnt(0)\n\ts_barrier" ::: "memory")
#define VMW(n) asm volatile("s_waitcnt vmcnt(%0)" :: "n"(n) : "memory")
template <int SL, int FW> __device__ __forceinline__ void poll4(f32x4 (&r)[4], u32x4 (&p)[12], const int (&f)[4], rsrc_t rs, Tag tag) {
  unsigned t, u, z, cnt;
  asm volatile(
      "s_mov_b32 %[cnt], %[mx]\n\t"
      ".if %[fw]\n\t"
      "buffer_load_dwordx4 v[64:67], %[f0], %[rs], 0 offen sc1\n\tbuffer_load_dwordx4 v[68:71], %[f1], %[rs], 0 offen sc1\n\tbuffer_load_dwordx4 v[72:75], %[f2], %[rs], 0 offen sc1\n\tbuffer_load_dwordx4 v[76:79], %[f3], %[rs], 0 offen sc1\n\t"
      "s_waitcnt vmcnt(0)\n\tv_xor_b32 v112, %[t0], v64\n\tv_xor_b32 v113, %[t1], v65\n\tv_xor_b32 v114, %[t2], v66\n\tv_xor_b32 v115, %[t3], v67\n\tv_xor_b32 v116, %[t0], v68\n\tv_xor_b32 v117, %[t1], v69\n\tv_xor_b32 v118, %[t2], v70\n\tv_xor_b32 v119, %[t3], v71\n\tv_xor_b32 v120, %[t0], v72\n\tv_xor_b32 v121, %[t1], v73\n\tv_xor_b32 v122, %[t2], v74\n\tv_xor_b32 v123, %[t3], v75\n\tv_xor_b32 v124, %[t0], v76\n\tv_xor_b32 v125, %[t1], v77\n\tv_xor_b32 v126, %[t2], v78\n\tv_xor_b32 v127, %[t3], v79\n\tv_or3_b32 %[t], v112, v113, v114\n\tv_or3_b32 %[u], v115, v116, v117\n\tv_or3_b32 %[z], v118, v119, v120\n\tv_or3_b32 %[t], %[t], v121, v122\n\tv_or3_b32 %[u], %[u], v123, v124\n\tv_or3_b32 %[z], %[z], v125, v126\n\tv_or3_b32 %[t], %[t], %[u], %[z]\n\tv_or_b32 %[t], %[t], v127\n\tv_lshlrev_b32 %[t], 22, %[t]\n\tv_cmp_ne_u32 vcc, 0, %[t]\n\ts_mov_b64 exec, vcc\n\ts_cbranch_execz 2%=f\n\t"
      ".endif\n\t"
      "buffer_load_dwordx4 v[64:67], %[f0], %[rs], 0 offen sc1\n\tbuffer_load_dwordx4 v[68:71], %[f1], %[rs], 0 offen sc1\n\tbuffer_load_dwordx4 v[72:75], %[f2], %[rs], 0 offen sc1\n\tbuffer_load_dwordx4 v[76:79], %[f3], %[rs], 0 offen sc1\n\ts_sleep %[sl]\n\t"
      "buffer_load_dwordx4 v[80:83], %[f0], %[rs], 0 offen sc1\n\tbuffer_load_dwordx4 v[84:87], %[f1], %[rs], 0 offen sc1\n\tbuffer_load_dwordx4 v[88:91], %[f2], %[rs], 0 offen sc1\n\tbuffer_load_dwordx4 v[92:95], %[f3], %[rs], 0 offen sc1\n\ts_sleep %[sl]\n\t"
      "1%=:\n\t"
      "buffer_load_dwordx4 v[96:99], %[f0], %[rs], 0 offen sc1\n\tbuffer_load_dwordx4 v[100:103], %[f1], %[rs], 0 offen sc1\n\tbuffer_load_dwordx4 v[104:107], %[f2], %[rs], 0 offen sc1\n\tbuffer_load_dwordx4 v[108:111], %[f3], %[rs], 0 offen sc1\n\t"
      "s_waitcnt vmcnt(8)\n\tv_xor_b32 v112, %[t0], v64\n\tv_xor_b32 v113, %[t1], v65\n\tv_xor_b32 v114, %[t2], v66\n\tv_xor_b32 v115, %[t3], v67\n\tv_xor_b32 v116, %[t0], v68\n\tv_xor_b32 v117, %[t1], v69\n\tv_xor_b32 v118, %[t2], v70\n\tv_xor_b32 v119, %[t3], v71\n\tv_xor_b32 v120, %[t0], v72\n\tv_xor_b32 v121, %[t1], v73\n\tv_xor_b32 v122, %[t2], v74\n\tv_xor_b32 v123, %[t3], v75\n\tv_xor_b32 v124, %[t0], v76\n\tv_xor_b32 v125, %[t1], v77\n\tv_xor_b32 v126, %[t2], v78\n\tv_xor_b32 v127, %[t3], v79\n\tv_or3_b32 %[t], v112, v113, v114\n\tv_or3_b32 %[u], v115, v116, v117\n\tv_or3_b32 %[z], v118, v119, v120\n\tv_or3_b32 %[t], %[t], v121, v122\n\tv_or3_b32 %[u], %[u], v123, v124\n\tv_or3_b32 %[z], %[z], v125, v126\n\tv_or3_b32 %[t], %[t], %[u], %[z]\n\tv_or_b32 %[t], %[t], v127\n\tv_lshlrev_b32 %[t], 22, %[t]\n\tv_cmp_ne_u32 vcc, 0, %[t]\n\ts_mov_b64 exec, vcc\n\ts_cbranch_execz 2%=f\n\t"
      "buffer_load_dwordx4 v[64:67], %[f0], %[rs], 0 offen sc1\n\tbuffer_load_dwordx4 v[68:71], %[f1], %[rs], 0 offen sc1\n\tbuffer_load_dwordx4 v[72:75], %[f2], %[rs], 0 offen sc1\n\tbuffer_load_dwordx4 v[76:79], %[f3], %[rs], 0 offen sc1\n\t"
      "s_waitcnt vmcnt(8)\n\tv_xor_b32 v112, %[t0], v80\n\tv_xor_b32 v113, %[t1], v81\n\tv_xor_b32 v114, %[t2], v82\n\tv_xor_b32 v115, %[t3], v83\n\tv_xor_b32 v116, %[t0], v84\n\tv_xor_b32 v117, %[t1], v85\n\tv_xor_b32 v118, %[t2], v86\n\tv_xor_b32 v119, %[t3], v87\n\tv_xor_b32 v120, %[t0], v88\n\tv_xor_b32 v121, %[t1], v89\n\tv_xor_b32 v122, %[t2], v90\n\tv_xor_b32 v123, %[t3], v91\n\tv_xor_b32 v124, %[t0], v92\n\tv_xor_b32 v125, %[t1], v93\n\tv_xor_b32 v126, %[t2], v94\n\tv_xor_b32 v127, %[t3], v95\n\tv_or3_b32 %[t], v112, v113, v114\n\tv_or3_b32 %[u], v115, v116, v117\n\tv_or3_b32 %[z], v118, v119, v120\n\tv_or3_b32 %[t], %[t], v121, v122\n\tv_or3_b32 %[u], %[u], v123, v124\n\tv_or3_b32 %[z], %[z], v125, v126\n\tv_or3_b32 %[t], %[t], %[u], %[z]\n\tv_or_b32 %[t], %[t], v127\n\tv_lshlrev_b32 %[t], 22, %[t]\n\tv_cmp_ne_u32 vcc, 0, %[t]\n\ts_mov_b64 exec, vcc\n\ts_cbranch_execz 2%=f\n\t"
      "buffer_load_dwordx4 v[80:83], %[f0], %[rs], 0 offen sc1\n\tbuffer_load_dwordx4 v[84:87], %[f1], %[rs], 0 offen sc1\n\tbuffer_load_dwordx4 v[88:91], %[f2], %[rs], 0 offen sc1\n\tbuffer_load_dwordx4 v[92:95], %[f3], %[rs], 0 offen sc1\n\t"
      "s_waitcnt vmcnt(8)\n\tv_xor_b32 v112, %[t0], v96\n\tv_xor_b32 v113, %[t1], v97\n\tv_xor_b32 v114, %[t2], v98\n\tv_xor_b32 v115, %[t3], v99\n\tv_xor_b32 v116, %[t0], v100\n\tv_xor_b32 v117, %[t1], v101\n\tv_xor_b32 v118, %[t2], v102\n\tv_xor_b32 v119, %[t3], v103\n\tv_xor_b32 v120, %[t0], v104\n\tv_xor_b32 v121, %[t1], v105\n\tv_xor_b32 v122, %[t2], v106\n\tv_xor_b32 v123, %[t3], v107\n\tv_xor_b32 v124, %[t0], v108\n\tv_xor_b32 v125, %[t1], v109\n\tv_xor_b32 v126, %[t2], v110\n\tv_xor_b32 v127, %[t3], v111\n\tv_or3_b32 %[t], v112, v113, v114\n\tv_or3_b32 %[u], v115, v116, v117\n\tv_or3_b32 %[z], v118, v119, v120\n\tv_or3_b32 %[t], %[t], v121, v122\n\tv_or3_b32 %[u], %[u], v123, v124\n\tv_or3_b32 %[z], %[z], v125, v126\n\tv_or3_b32 %[t], %[t], %[u], %[z]\n\tv_or_b32 %[t], %[t], v127\n\tv_lshlrev_b32 %[t], 22, %[t]\n\tv_cmp_ne_u32 vcc, 0, %[t]\n\ts_mov_b64 exec, vcc\n\ts_cbranch_execz 2%=f\n\t"
      "s_sub_u32 %[cnt], %[cnt], 1\n\ts_cmp_lg_u32 %[cnt], 0\n\ts_cbranch_scc1 1%=b\n2%=:\n\ts_mov_b64 exec, -1"
      : "=&{v[112:115]}"(r[0]), "=&{v[116:119]}"(r[1]), "=&{v[120:123]}"(r[2]), "=&{v[124:127]}"(r[3]),
        "=&{v[64:67]}"(p[0]), "=&{v[68:71]}"(p[1]), "=&{v[72:75]}"(p[2]), "=&{v[76:79]}"(p[3]), "=&{v[80:83]}"(p[4]), "=&{v[84:87]}"(p[5]), "=&{v[88:91]}"(p[6]), "=&{v[92:95]}"(p[7]), "=&{v[96:99]}"(p[8]), "=&{v[100:103]}"(p[9]), "=&{v[104:107]}"(p[10]), "=&{v[108:111]}"(p[11]),
        [t] "=&v"(t), [u] "=&v"(u), [z] "=&v"(z), [cnt] "=&s"(cnt)
      : [f0] "v"(f[0]), [f1] "v"(f[1]), [f2] "v"(f[2]), [f3] "v"(f[3]), [rs] "s"(rs), [t0] "s"(tag.t[0]), [t1] "s"(tag.t[1]), [t2] "s"(tag.t[2]), [t3] "s"(tag.t[3]), [mx] "n"(SPIN_MAX), [sl] "n"(SL), [fw] "n"(FW)
      : "vcc", "scc", "memory");
}
__device__ __forceinline__ void poll4_keep(u32x4 (&p)[12]) { asm volatile("" :: "{v[64:67]}"(p[0]), "{v[68:71]}"(p[1]), "{v[72:75]}"(p[2]), "{v[76:79]}"(p[3]), "{v[80:83]}"(p[4]), "{v[84:87]}"(p[5]), "{v[88:91]}"(p[6]), "{v[92:95]}"(p[7]), "{v[96:99]}"(p[8]), "{v[100:103]}"(p[9]), "{v[104:107]}"(p[10]), "{v[108:111]}"(p[11])); }
#define LDSWAITV(fs_, word, n, slp) do { unsigned fv_; \
    asm volatile("1%=:\n\tds_read_b32 %0, %2\n\ts_waitcnt lgkmcnt(0)\n\tv_readfirstlane_b32 %1, %0\n\ts_cmp_lt_u32 %1, %3\n\ts_cbranch_scc0 2%=f\n\ts_sleep %4\n\ts_branch 1%=b\n2%=:" \
                 : "=&v"(fv_), "=&s"(fs_) : "v"((unsigned)(uintptr_t)&(word)), "n"(n), "n"(slp) : "scc", "memory"); } while (0)
#define LDSINC(word) asm volatile("s_mov_b64 exec, 1\n\tds_add_u32 %0, %1\n\ts_mov_b64 exec, -1" :: "v"((unsigned)(uintptr_t)&(word)), "v"(1u) : "memory")
#define LDSWAIT(word, n, slp) do { unsigned fs0_; LDSWAITV(fs0_, word, n, slp); } while (0)

#define LAND(n) asm volatile(".Lland%0_" #n ":\n\ts_cbranch_execz .Lback%0" :: "n"(UID))
constexpr int ASTR = 132;

__global__ void __attribute__((aligned(32768))) __launch_bounds__(768, 3)
fused_k(const bf16* __restrict__ x, const bf16* __restrict__ kc, const bf16* __restrict__ vc, const bf16* __restrict__ wq,
        const bf16* __restrict__ wo, const bf16* __restrict__ w1, const bf16* __restrict__ w3, const bf16* __restrict__ w2,
        unsigned char* __restrict__ ws, bf16* __restrict__ out) {
  __shared__ __attribute__((aligned(16))) f32x4 red[16][8][9];
  __shared__ __attribute__((aligned(16))) float stat[NB][2];
  __shared__ __attribute__((aligned(16))) unsigned qs[2][DH / 2];
  __shared__ __attribute__((aligned(16))) unsigned clk[8];
  __shared__ __attribute__((aligned(16))) unsigned ctl[20];
  __shared__ __attribute__((aligned(16))) float mrg[2][8][DH];
  __shared__ __attribute__((aligned(16))) float mlw[3][16];
  __shared__ __attribute__((aligned(16))) unsigned oA[NB][ASTR];
  __shared__ __attribute__((aligned(16))) u32x4 wf[6][8][64];
  asm volatile("s_cmp_gt_u32 %1, 255\n\ts_cbranch_scc1 .Lback%0\n\ts_mov_b64 exec, 0\n\ts_cmp_lt_u32 %1, 64\n\ts_cbranch_scc1 .Lland%0_0\n\t"
               "s_cmpk_lt_u32 %1, 128\n\ts_cbranch_scc1 .Lland%0_1\n\ts_cmpk_lt_u32 %1, 192\n\ts_cbranch_scc1 .Lland%0_2\n\ts_branch .Lland%0_3\n"
               ".Lback%0:\n\ts_mov_b64 exec, -1" :: "n"(UID), "s"(__builtin_amdgcn_readfirstlane(threadIdx.x)) : "scc");
  SB();
  u64 pc0;
  asm volatile("s_getpc_b64 %0" : "=s"(pc0));
  asm volatile("" :: "s"(x), "s"(kc), "s"(vc), "s"(wq), "s"(wo), "s"(w1), "s"(w3), "s"(w2), "s"(ws), "s"(out));
#define HOF(b) (ALT == 1 ? ((b) & 24) | (((b) & 3) << 1) | (((b) >> 2) & 1) : ALT == 2 ? ((b) & 25) | (((b) & 2) << 1) | (((b) >> 1) & 2) : ALT == 3 ? ((b) & 31) ^ 4 : (b) & 31)
#define ROF(h) (RB == 1 ? ((h) >= 18 && (h) < 22 ? (h) ^ 6 : (h) == 27 || (h) == 30 ? (h) ^ 5 : (h)) : RB == 2 ? ((h) >= 16 && (h) < 24 ? (h) ^ 4 : (h)) : (h))
  const int h = HOF(blockIdx.x), sp = blockIdx.x >> 5, pr = sp & 1, mem = sp >> 1;
  const int wi = AO ? 2 * h + pr : NH * pr + h;
#define PHS (AO == 2 ? (wi * SPAN) >> 6 : (h * SPAN) >> 5)
  const int tid = threadIdx.x, lane = tid & 63, wave = __builtin_amdgcn_readfirstlane(tid >> 6);
  const rsrc_t rw = mkrs(ws, WS_END);
  Tag tag;
  const bool proj = ROF(h) < NPH, ffn = ROF(h) >= NPH && ROF(h) < NPH + 10;
#define ROLES() int bx_ = blockIdx.x; asm volatile("" : "+s"(bx_)); \
  const int ru8 = 8 * (ROF(HOF(bx_)) - (proj ? 0 : NPH)) + (bx_ >> 5), grp = (ru8 * 205) >> 10, cb = ru8 - 5 * grp; (void)cb
  if (__builtin_expect(wave >= 4, 1)) {
    const int w = wave - 4, kk = lane >> 3, sub = lane & 7;
    const size_t wbase = ((size_t)wi * SK + (SK - WIN)) * DH;
    const rsrc_t rk = mkrs(kc + wbase, BSTR + WIN * DH * 2), rv = mkrs(vc + wbase, BSTR + WIN * DH * 2);
    const int voff = ((mem * 8 + w) * 8 + kk) * (DH * 2) + 16 * sub;
    u32x4 tl[NBUF][3];
    u64 tst0;
    asm volatile("s_memrealtime %0" : "=s"(tst0));
    unsigned tstart;
    unsigned late = 0, dstep = 0;
    unsigned xd = 0, sl = 0;
#define PACE(t) do { if ((t) >= PF) { \
      if ((t) >= NBUF && (t) < NBUF + (1 << RSH)) tstart += (((int)dstep * ((t) - NBUF + 1)) >> RSH) - (((int)dstep * ((t) - NBUF)) >> RSH); \
      { unsigned xv_; asm volatile("s_add_u32 %0, %0, %1" : "+s"(tstart) : "s"(sl) : "scc");     \
      asm volatile("1%=:\n\tv_mov_b32 %1, %6\n\tds_read_b32 %1, %1\n\ts_memrealtime s[90:91]\n\ts_waitcnt lgkmcnt(0)\n\tv_readfirstlane_b32 %2, %1\n\ts_sub_u32 %0, s90, %4\n\t" \
                   "s_and_b32 %3, %2, 255\n\ts_lshr_b32 %2, %2, 8\n\ts_addk_i32 %0, %5\n\ts_sub_u32 %0, %0, %2\n\t" \
                   "s_cmp_lt_i32 %0, 0\n\ts_cbranch_scc0 2%=f\n\ts_sleep 1\n\ts_branch 1%=b\n2%=:" \
                   : "=&s"(late), "=&v"(xv_), "=&s"(xd), "=&s"(sl) : "s"(tstart), "n"(-sched(t)), "s"((unsigned)(uintptr_t)&ctl[14]) : "scc", "s90", "s91", "memory"); \
      } \
      if (ckof(t) >= 0) { unsigned oc_;     \
        asm volatile("s_getreg_b32 %0, hwreg(HW_REG_IB_STS, 0, 4)" : "=s"(oc_)); \
        if (ckof(t) >= 0) asm volatile("s_mov_b64 exec, 1\n\tds_add_u32 %0, %1\n\ts_mov_b64 exec, -1" :: "v"((unsigned)(uintptr_t)&ctl[16 + (ckof(t) & 3)]), "v"(PCAP - min(10 * oc_ + late, (unsigned)(ckof(t) == 0 ? CP0 : ckof(t) == 1 ? CP1 : CP2))) : "memory"); \
      } \
      if (PR1) asm volatile("s_setprio 0\n\ts_cmpk_lt_u32 %0, %1\n\ts_cbranch_scc1 1%=f\n\ts_setprio 1\n\ts_cmpk_lt_u32 %0, %2\n\ts_cbranch_scc1 1%=f\n\t" \
                   "s_setprio 2\n\ts_cmpk_lt_u32 %0, %3\n\ts_cbranch_scc1 1%=f\n\ts_setprio 3\n1%=:" \
                   :: "s"(late), "n"(PR1), "n"(PR2), "n"(PR3) : "scc", "memory"); } } while (0)
#define WINDOW(t) do { if (LATE && (t) >= PF && (t) < TSW) \
      asm volatile("s_cmpk_lt_u32 %0, %1\n\ts_cbranch_scc0 1%=f\n\ts_waitcnt vmcnt(%2)\n1%=:\n\ts_waitcnt vmcnt(%3)" \
                   :: "s"(late), "n"(LATE), "n"(WINL - 1), "n"(WINB - 1) : "scc", "memory"); \
      else VMW(((t) < TSW ? WINL : WINV) - 1); } while (0)
#define ISSUE_T(t) do { PACE(t); _Pragma("unroll") for (int i_ = 0; i_ < 3; ++i_) { \
      WINDOW(t); \
      tl[(t) % NBUF][i_] = ldb_nt((t) < NT ? rk : rv, voff + 128 * i_, ((((t) >> 3) & 1) ^ 1) * BSTR + ((t) & 7) * TSTR); \
      SB(); } } while (0)
    SB();
    ISSUE_T(0);
    asm volatile("s_waitcnt lgkmcnt(0)" : "+s"(tst0));
    clk[w] = (unsigned)tst0;
    int tfst = 0;
    if (w == 0) { VMW(2); tfst = (int)((unsigned)__builtin_amdgcn_s_memrealtime() - (unsigned)tst0); }
    BAR();
    tstart = __builtin_amdgcn_readfirstlane(clk[1]) + TOFS + PHS;
    if (w == 0) ctl[14] = (unsigned)(min(max((tfst - L0) * LGN >> 3, 0), RMX) << 8);
#pragma unroll
    for (int t = 1; t < NBUF; ++t) ISSUE_T(t);
    SB();
    LDSWAIT(ctl[13], 1, 2);
    dstep = __builtin_amdgcn_readfirstlane(ctl[12]);
    u32x4 qp[3];
    float sc[NT];
#pragma unroll
    for (int t = 0; t < NT; ++t) {
      SB();
      if ((t & 7) == 0) {
#pragma unroll
        for (int i = 0; i < 3; ++i) qp[i] = *reinterpret_cast<const u32x4*>(&qs[(t >> 3) ^ 1][32 * i + 4 * sub]);
      }
      float d0 = 0.f, d1 = 0.f;
#pragma unroll
      for (int i = 0; i < 3; ++i) {
        d0 = dot2(tl[t % NBUF][i][0], qp[i][0], d0); d1 = dot2(tl[t % NBUF][i][1], qp[i][1], d1);
        d0 = dot2(tl[t % NBUF][i][2], qp[i][2], d0); d1 = dot2(tl[t % NBUF][i][3], qp[i][3], d1);
      }
      asm volatile("" : "+v"(d0), "+v"(d1));
      SB();
      ISSUE_T(t + NBUF);
      SB();
      float d = d0 + d1;
      d += dpp<XOR1>(d); d += dpp<XOR2>(d); d += dpp<XOR7>(d);
      sc[t ^ 8] = rbf(rbf(d) * SCALE);
    }
    float m[2];
#pragma unroll
    for (int bi = 0; bi < 2; ++bi) {
      float mm = sc[8 * bi], l = 0.f;
#pragma unroll
      for (int t = 1; t < 8; ++t) mm = fmaxf(mm, sc[8 * bi + t]);
      mm = fmaxf(mm, dpp<XOR8>(mm)); mm = x16max(mm); mm = x32max(mm);
      m[bi] = mm * LOG2E;
#pragma unroll
      for (int t = 8 * bi; t < 8 * bi + 8; ++t) { sc[t] = __builtin_amdgcn_exp2f(fmaf(sc[t], LOG2E, -m[bi])); l += sc[t]; }
      mlw[0][8 * bi + w] = m[bi]; mlw[1][8 * bi + w] = sum_kk(l);
    }
    LDSINC(ctl[4]);
    u32x4 msk;
#pragma unroll
    for (int r = 0; r < 4; ++r) msk[r] = (sub >> 1) == r ? ((sub & 1) ? 0xffff0000u : 0x0000ffffu) : 0u;
    f32x4 acc[3];
    unsigned mwr = (unsigned)(uintptr_t)&mrg[0][w][32 * ((lane >> 4) & 1) + 4 * sub + 2 * (lane >> 5)];
    unsigned mrd = (unsigned)(uintptr_t)&mrg[1][0][4 * (lane < 48 ? lane : 0)];
    int opub = lane < 48 ? WS_O + (((wi & 31) * NB + (wi >> 5)) * 4 + mem) * (DH * 4) + 16 * ((lane & 48) + 2 * sub + ((lane >> 3) & 1)) : WS_END;
#define DEPOSIT(tick, ...) do { float a_[12]; _Pragma("unroll") for (int i_ = 0; i_ < 12; ++i_) a_[i_] = acc[i_ >> 2][i_ & 3]; \
      asm volatile("s_nop 5\n\tv_permlane32_swap_b32 %0, %2\n\tv_permlane32_swap_b32 %1, %3\n\tv_permlane32_swap_b32 %4, %6\n\tv_permlane32_swap_b32 %5, %7\n\t" \
                   "v_permlane32_swap_b32 %8, %10\n\tv_permlane32_swap_b32 %9, %11\n\t" \
                   "v_add_f32_dpp %0, %2, %0 row_ror:8 row_mask:0xf bank_mask:0xf\n\tv_add_f32_dpp %1, %3, %1 row_ror:8 row_mask:0xf bank_mask:0xf\n\t" \
                   "v_add_f32_dpp %4, %6, %4 row_ror:8 row_mask:0xf bank_mask:0xf\n\tv_add_f32_dpp %5, %7, %5 row_ror:8 row_mask:0xf bank_mask:0xf\n\t" \
                   "v_add_f32_dpp %8, %10, %8 row_ror:8 row_mask:0xf bank_mask:0xf\n\tv_add_f32_dpp %9, %11, %9 row_ror:8 row_mask:0xf bank_mask:0xf\n\t" \
                   "s_mov_b64 exec, %[vm]\n\tds_write2_b32 %[a], %0, %1 offset1:1\n\tds_write2_b32 %[a], %4, %5 offset0:64 offset1:65\n\tds_write2_b32 %[a], %8, %9 offset0:128 offset1:129\n\t" \
                   "s_mov_b64 exec, 1\n\t" tick "\n\ts_mov_b64 exec, -1" \
                   : "+v"(a_[0]), "+v"(a_[1]), "+v"(a_[2]), "+v"(a_[3]), "+v"(a_[4]), "+v"(a_[5]), "+v"(a_[6]), "+v"(a_[7]), "+v"(a_[8]), "+v"(a_[9]), "+v"(a_[10]), "+v"(a_[11]), [o] "=&v"(old) \
                   : [vm] "s"(0x00ff00ff00ff00ffull), [a] "v"(mwr), [one] "v"(1u), __VA_ARGS__ : "memory"); } while (0)
    unsigned old;
#pragma unroll
    for (int i = 0; i < 3; ++i) acc[i] = f32x4{0.f, 0.f, 0.f, 0.f};
#pragma unroll
    for (int t = 0; t < NT; ++t) {
      SB();
      if (t == 7) {
        LDSWAIT(ctl[4], 8, 1);
        tag = mktag(__builtin_amdgcn_readfirstlane(ctl[0]));
#pragma unroll
        for (int bi = 0; bi < 2; ++bi) {
          const f32x4 ma = *reinterpret_cast<const f32x4*>(&mlw[0][8 * bi]), mb = *reinterpret_cast<const f32x4*>(&mlw[0][8 * bi + 4]);
          const float M = rnd22(fmaxf(fmaxf(fmaxf(ma[0], ma[1]), fmaxf(ma[2], ma[3])), fmaxf(fmaxf(mb[0], mb[1]), fmaxf(mb[2], mb[3]))));
          const float wg = __builtin_amdgcn_exp2f(m[bi] - M);
          if (bi == 1) {
#pragma unroll
            for (int i = 0; i < 3; ++i) acc[i] *= wg;
          }
#pragma unroll
          for (int u = bi ? 15 : 0; u < 8 * bi + 8; ++u) sc[u] *= wg;
        }
        asm volatile("" : "+v"(mwr), "+v"(mrd), "+v"(opub), "+s"(tag.t[0]), "+s"(tag.t[1]), "+s"(tag.t[2]), "+s"(tag.t[3]));
        SB();
      }
      const unsigned pd = cvtpk(sc[t ^ 8], sc[t ^ 8]);
      const u32x4 af = {pd & msk[0], pd & msk[1], pd & msk[2], pd & msk[3]};
#pragma unroll
      for (int i = 0; i < 3; ++i) acc[i] = mfma(af, tl[(t + NT) % NBUF][i], acc[i]);
      SB();
      if (t == 7) {
        DEPOSIT("ds_add_u32 %[c], %[one]", [c] "v"((unsigned)(uintptr_t)&ctl[10]));
        mwr += 8 * DH * 4;
#pragma unroll
        for (int i = 0; i < 3; ++i) acc[i] = f32x4{0.f, 0.f, 0.f, 0.f};
      }
      if (viss(t)) ISSUE_T(viss(t));
      if (viss(t) == WTRIG) { unsigned tv_; asm volatile("s_add_u32 %1, %1, %2\n\tv_mov_b32 %0, %1" : "=v"(tv_), "+s"(tstart) : "s"(xd) : "scc"); clk[w] = tv_; LDSINC(ctl[8]); }
      SB();
    }
    {
      f32x4 pv[8];
      DEPOSIT("ds_add_rtn_u32 %[o], %[c], %[one]", [c] "v"((unsigned)(uintptr_t)&ctl[5]));
      typedef __attribute__((address_space(3))) const f32x4* lp4;
      asm volatile("s_waitcnt lgkmcnt(0)" : "+v"(old) :: "memory");
      const unsigned tk = __builtin_amdgcn_readfirstlane(old);
      if (tk == 7u || tk == ET) {
        if (__builtin_expect(tk == ET, 0)) { LDSWAIT(ctl[10], 8, 1); mrd -= 8 * DH * 4; opub += 2 * 4 * DH * 4; }
#pragma unroll
        for (int i = 0; i < 8; ++i) pv[i] = *reinterpret_cast<lp4>(mrd + i * (DH * 4));
        SB();
#pragma unroll
        for (int st = 1; st < 8; st *= 2)
#pragma unroll
          for (int i = 0; i < 8; i += 2 * st) pv[i] += pv[i + st];
        st_ag(rw, opub, g_pack(pv[0][0], pv[0][1], pv[0][2], pv[0][3], tag));
      }
    }
    if (proj || ffn) {
      ROLES();
      const int j = lane & 15, g = lane >> 4;
      typedef __attribute__((address_space(3))) const u32x4* lpu;
      const int wd = (cb * 8 + w) * 64 + lane;
      unsigned ydst = WS_Y + (wd / YP) * YL + (wd % YP) * 8;
      unsigned oo = (g * DM + 128 * cb + 8 * j + w) * 2;
      unsigned ard = (unsigned)(uintptr_t)&oA[j >> 2][4 * g + 16 * (j & 3)], brd = (unsigned)(uintptr_t)&wf[0][w][lane];
      u32x4 bw[6], af[2];
      f32x4 y[3];
      auto finish = [&](const float yv) __attribute__((always_inline)) {
        u64 add = __builtin_bit_cast(u64, __builtin_fma((double)yv, 274877906944.0, 6755399441055744.0)) + ((1ull << 57) - 0x4338000000000000ull);
        const bool side = (__float_as_uint(yv) << 1) - (YLO << 24) > ((YHI - YLO) << 24);
        asm volatile("" : "+v"(add));
        if (__builtin_expect(__builtin_amdgcn_ballot_w64(side) != 0, 0)) {
          if (side) {
            __builtin_amdgcn_raw_buffer_store_b64(u32x2{__float_as_uint(yv), ctl[0]}, rw, WS_S + ((proj ? grp : NH + grp) * (NB * DM) + wd) * 8, 0, AUX_SC1);
            __hip_atomic_fetch_or(reinterpret_cast<u64*>(ws + ydst), 1ull << 63, __ATOMIC_RELAXED, __HIP_MEMORY_SCOPE_AGENT);
            add = 1ull << 57;
          }
          VMW(0);
          asm volatile("" : "+v"(ydst));
        }
        u64 tot = __hip_atomic_fetch_add(reinterpret_cast<u64*>(ws + ydst), add, __ATOMIC_RELAXED, __HIP_MEMORY_SCOPE_AGENT);
        SB();
        add += (1ull << 31) - ((u64)NY << 57);
        asm volatile("" : "+v"(add));
        SB();
        tot += add;
        asm volatile("" : "+v"(tot));
        const int hi = (int)(tot >> 31) >> 1;
        if ((unsigned)(hi + (1 << 24)) < (1u << 25)) {
          float z = fmaf((float)hi, 1.f / 64, (float)(int)((unsigned)tot ^ 0x80000000u) * (1.f / 274877906944.f));
          if (__builtin_expect(hi != (int)(tot >> 32), 0)) {
#pragma unroll 1
            for (int a = 0; a < NY; ++a) {
              const u32x2 v = __builtin_amdgcn_raw_buffer_load_b64(rw, WS_S + (a * (NB * DM) + wd) * 8, 0, AUX_SC1);
              if (v[1] == ctl[0]) z += __uint_as_float(v[0]);
            }
          }
          *reinterpret_cast<unsigned short*>(reinterpret_cast<unsigned char*>(out) + oo) = (unsigned short)cvtpk(z, 0.f);
        }
      };
      if (proj && w < 3) {
        const int bb = lane >> 4, dq = 16 * w + (lane & 15);
        const int f0 = WS_O + (grp * NB + bb) * 4 * (DH * 4) + 16 * dq;
        int f[4] = {f0, f0 + DH * 4, f0 + 2 * DH * 4, f0 + 3 * DH * 4};
        unsigned awr = (unsigned)(uintptr_t)&oA[bb][16 * (w + 3 * ((lane >> 3) & 1)) + 2 * (lane & 7)];
        f32x4 r[4]; u32x4 p[12];
        LDSWAIT(ctl[9], 1, 1);
        f32x4 sq = *reinterpret_cast<const f32x4*>(&mlw[2][4 * bb]);
        asm volatile("" : "+v"(sq), "+v"(awr), "+v"(ydst), "+v"(ard), "+v"(brd), "+v"(oo), "+v"(f[0]), "+v"(f[1]), "+v"(f[2]), "+v"(f[3]));
        tstart += sched(31) - PHS + max(PT1 + (((AO ? 32 + grp : 2 * grp) * SPAN) >> 6), WB + PD + ((grp * WPB) >> 5));
        while ((int)((unsigned)__builtin_amdgcn_s_memrealtime() - tstart) < 0) __builtin_amdgcn_s_sleep(1);
        poll4<PSL1, FW1>(r, p, f, rw, tag);
        const f32x4 o = (sq[0] * r[0] + sq[1] * r[1]) + (sq[2] * r[2] + sq[3] * r[3]);
        *reinterpret_cast<__attribute__((address_space(3))) u32x2*>(awr) = u32x2{cvtpk(o[0], o[1]), cvtpk(o[2], o[3])};
        BAR();
        af[0] = *reinterpret_cast<lpu>(ard); af[1] = *reinterpret_cast<lpu>(ard + 192);
#pragma unroll
        for (int ks = 0; ks < 6; ++ks) bw[ks] = *reinterpret_cast<lpu>(brd + 8192 * ks);
        SB();
#pragma unroll
        for (int i = 0; i < 3; ++i) y[i] = mfma(af[0], bw[i], f32x4{0.f, 0.f, 0.f, 0.f});
#pragma unroll
        for (int i = 0; i < 3; ++i) y[i] = mfma(af[1], bw[3 + i], y[i]);
        finish((y[0][0] + y[1][1]) + y[2][2]);
        poll4_keep(p);
      } else {
        LDSWAIT(ctl[15], 3, 1);
#pragma unroll
        for (int ks = 0; ks < 3; ++ks) bw[ks] = *reinterpret_cast<lpu>(brd + 8192 * ks);
#pragma unroll
        for (int ks = 0; ks < 3; ++ks) asm volatile("" : "+v"(bw[ks]));
        LDSWAIT(ctl[6], 4, 0);
        if (proj) {
#pragma unroll
          for (int ks = 3; ks < 6; ++ks) bw[ks] = *reinterpret_cast<lpu>(brd + 8192 * ks);
#pragma unroll
          for (int ks = 3; ks < 6; ++ks) asm volatile("" : "+v"(bw[ks]));
          asm volatile("" : "+v"(ydst), "+v"(ard), "+v"(oo));
          BAR();
          af[0] = *reinterpret_cast<lpu>(ard); af[1] = *reinterpret_cast<lpu>(ard + 192);
          SB();
#pragma unroll
          for (int i = 0; i < 3; ++i) y[i] = mfma(af[0], bw[i], f32x4{0.f, 0.f, 0.f, 0.f});
#pragma unroll
          for (int i = 0; i < 3; ++i) y[i] = mfma(af[1], bw[3 + i], y[i]);
        } else {
          const float xs = bflo(reinterpret_cast<const unsigned short*>(&red[0][0][0])[8 * lane + w]);
          af[0] = *reinterpret_cast<lpu>(ard);
#pragma unroll
          for (int i = 0; i < 3; ++i) y[i] = mfma(af[0], bw[i], f32x4{i == 0 ? xs : 0.f, 0.f, 0.f, 0.f});
        }
        finish((y[0][0] + y[1][1]) + y[2][2]);
      }
    }
  } else {
    LAND(0);
    const int w = wave;
    unsigned tstart;
    const unsigned town = (unsigned)__builtin_amdgcn_s_memrealtime() + SOFS;
    if (PRS) __builtin_amdgcn_s_setprio(PRS);
    const rsrc_t rc = mkrs(reinterpret_cast<const void*>(pc0 & ~255ull), CODEB);
    const int cofs = 1024 * (blockIdx.x >> 3);
    u32x4 cv = {0u, 0u, 0u, 0u};
    if (CPE && w == 3) cv = ldb(rc, 16 * lane, cofs);
    {
      const int j = lane & 15, g = lane >> 4;
      const bool hq = (h ^ (h >> HQX)) & 1;
      const bool isq = hq || sp >= 4;
      const int fi = sp, cb0 = 3 * (h >> 1);
      const int qu = hq ? sp + 4 : sp - 4, qh = (h & 30) + (qu >= 6), qcol = qh * DH + 64 * ((qu >= 6 ? qu - 6 : qu) >> 1);
      const int ns = isq ? 5 : 8 - (fi & 1), n1 = isq ? 5 : (0x7528 >> (4 * fi)) & 15, s0 = isq ? 5 * (qu & 1) : (15 * fi + 1) >> 1;
      const int ld2 = isq ? NH * DH * 2 : FF * 2;
      const rsrc_t rx = mkrs(x, NB * DM * 2);
      const int vo = (32 * (j >> 3) + 8 * g) * ld2 + 16 * (j & 7), xo = ((j & 3) * DM + 32 * ((j >> 2) & 1) + 8 * g) * 2;
      u32x4 wv[2][8], xa[2], xr[2];
      LAND(1);
      if (ZE && (blockIdx.x >> 3) == 16 && lane < 40) {
        const int wd = 320 * (blockIdx.x & 7) + 80 * w + 2 * lane;
        st_ag(rw, WS_Y + (wd / YP) * YL + (wd % YP) * 8, u32x4{0u, 0u, 0u, 0u});
      }
      const u32x2 ep = __builtin_amdgcn_raw_buffer_load_b64(rw, WS_EPOCH, 0, AUX_SC1);
#pragma unroll
      for (int i = 0; i < 2; ++i) xr[i] = ldb(rx, (w * DM + 16 * (lane < 40 ? lane : 0) + 8 * i) * 2, 0);
#pragma unroll
      for (int st = 0; st < 2; ++st) {
        const int gs = s0 + w + 4 * st, cw = cb0 + (gs >= 10) + (gs >= 20), rs = gs - 10 * ((gs >= 10) + (gs >= 20));
        xa[st] = ldb(rx, xo, 128 * rs);
        if (st == 1) LAND(2);
        if (st == 0 || w + 4 < ns) {
          const bf16* um = isq ? wq + qcol : (cw < 24 ? w1 + 64 * cw : w3 + 64 * (cw - 24));
          const rsrc_t ru = mkrs(um, (unsigned)(DM - 1) * ld2 + 128);
#pragma unroll
          for (int e = 0; e < 8; ++e) { wv[st][e] = ldb_nt(ru, vo, (64 * rs + e) * ld2); SB(); }
        }
      }
      LAND(3);
      if (!ZE && (blockIdx.x >> 3) == 16 && lane < 40) {
        const int wd = 320 * (blockIdx.x & 7) + 80 * w + 2 * lane;
        st_ag(rw, WS_Y + (wd / YP) * YL + (wd % YP) * 8, u32x4{0u, 0u, 0u, 0u});
      }
      if (tid < 18) ctl[2 + tid] = 0u;
      SB();
      VMW(12);
      BAR();
      SB();
      const unsigned tst0 = clk[1];
      const u64 epoch = ((u64)__builtin_amdgcn_readfirstlane(ep[0]) | ((u64)__builtin_amdgcn_readfirstlane(ep[1]) << 32)) + 1ull;
      tag = mktag((unsigned)epoch);
      u32x4 stg = s_tag((unsigned)epoch, lane);
      const int da = isq ? WS_Q + ((qu & 1) * (NH * DH) + qcol) * 8 : WS_GU + ((fi > 0 ? 2 * FF : 0) + 64 * (cb0 + (fi > 0 ? fi - 1 : 0))) * 8;
      int dst = (lane < 32 ? da : n1 < ns ? WS_GU + 64 * (cb0 + fi) * 8 : WS_END) + 16 * (lane & 31);
      unsigned rdr = (unsigned)(uintptr_t)&red[lane >> 5][2 * (lane & 3)][(lane & 31) >> 2];
      asm volatile("" : "+v"(stg), "+v"(dst), "+v"(rdr), "+s"(tag.t[0]), "+s"(tag.t[1]), "+s"(tag.t[2]), "+s"(tag.t[3]));
      {
        float v[16]; float sm = 0.f;
#pragma unroll
        for (int i = 0; i < 2; ++i)
#pragma unroll
          for (int k = 0; k < 4; ++k) { v[8 * i + 2 * k] = bflo(xr[i][k]); v[8 * i + 2 * k + 1] = bfhi(xr[i][k]); sm += v[8 * i + 2 * k] + v[8 * i + 2 * k + 1]; }
        const float mean = wave_sum(lane < 40 ? sm : 0.f) * (1.f / DM);
        float sq = 0.f;
#pragma unroll
        for (int k = 0; k < 16; ++k) { v[k] -= mean; sq += v[k] * v[k]; }
        const float var = wave_sum(lane < 40 ? sq : 0.f) * (1.f / DM);
        stat[w][0] = rbf(mean); stat[w][1] = 1.f / rbf(sqrtf(rbf(rbf(var) + 1e-5f)));
        LDSINC(ctl[7]);
        LDSWAIT(ctl[7], 4, 0);
      }
      SB();
      const float mean = stat[j & 3][0], rinv = stat[j & 3][1];
#pragma unroll
      for (int st = 0; st < 2; ++st) {
        if (st == 0 || w + 4 < ns) {
          u32x4 af;
#pragma unroll
          for (int p = 0; p < 4; ++p) {
            const unsigned c = cvtpk(bflo(xa[st][p]) - mean, bfhi(xa[st][p]) - mean);
            af[p] = cvtpk(bflo(c) * rinv, bfhi(c) * rinv);
          }
          f32x4 d[8];
#pragma unroll
          for (int ec = 0; ec < 8; ++ec) d[ec] = mfma(af, bfrag(&wv[st][0], ec), f32x4{0.f, 0.f, 0.f, 0.f});
          if (g == (j >> 3)) {
#pragma unroll
            for (int ec = 0; ec < 8; ++ec) red[2 * (w + 4 * st) + g][ec][j & 7] = d[ec];
          }
        }
      }
      tstart = __builtin_amdgcn_readfirstlane(tst0);
      asm volatile("" :: "v"(cv));
      if (tid == 0) *reinterpret_cast<u64*>(&ctl[0]) = epoch;
      asm volatile("s_waitcnt vmcnt(0) lgkmcnt(0)" ::: "memory");
      unsigned old = 0;
      if (lane == 0) old = __hip_atomic_fetch_add(&ctl[2], 1u, __ATOMIC_RELAXED, __HIP_MEMORY_SCOPE_WORKGROUP);
      const bool last = __builtin_amdgcn_readfirstlane(old) == 3u;
      FENCE();
      if (__builtin_amdgcn_readfirstlane(old) == 0u) {
        typedef const unsigned char* cp;
        ROLES();
        const size_t wb = ((size_t)wi * SK + (SK - WIN)) * DH * 2 + (j & 8) * 49152 + (j & 1) * 393088;
        const cp tp = g == 2 && TOL >= 0 ? ws + WS_O + (((wi & 31) * NB + (wi >> 5) + (j < 6 ? 0 : 2)) * 4 + mem) * (DH * 4) + TOL * (j < 6 ? j : j < 12 ? j - 6 : 0)
                    : g == 3 ? (j < 15 || !TCK ? ws + WS_Y + ((j & TYM) * (WS_S - WS_Y) / 15 & ~255) + 192 : ws + WS_CK + (TCK == 2 ? (blockIdx.x >> 4 & 3) * 8192 + (blockIdx.x & 15) * 128 : blockIdx.x * 128))
                    : mem == 0 ? (cp)kc + wb : mem == 1 ? (cp)vc + wb : mem == 2 ? (cp)vc + wb + BSTR
                    : j < 8 && proj ? (cp)wo + min(DH * (((ru8 & ~7) * 205) >> 10) + 36 * ((2 * j + pr) & TWM), NH * DH - 1) * (DM * 2)
                    : j < 8 && ffn ? (cp)w2 + min(96 * (((ru8 & ~7) * 205) >> 10) + 18 * ((2 * j + pr) & TWM), FF - 1) * (DM * 2)
                    : ws + WS_O + min(j & TOM, 5) * 65536 + 65472;
        unsigned tv;
        asm volatile("global_load_dword %0, %1, off sc1\n\ts_waitcnt vmcnt(0)" : "=&v"(tv) : "v"(tp) : "memory");
      }
      if (last) {
        {
          typedef __attribute__((address_space(3))) const f32x4* lp4;
          u32x4 gs;
          f32x4 v[8][2], va[2], vb[2] = {};
#pragma unroll
          for (int p = 0; p < 5; ++p) { v[p][0] = *reinterpret_cast<lp4>(rdr + 2304 * p); v[p][1] = *reinterpret_cast<lp4>(rdr + 2304 * p + 144); }
          if (isq) {
            SB();
#pragma unroll
            for (int k = 0; k < 2; ++k) va[k] = ((v[0][k] + v[1][k]) + (v[2][k] + v[3][k])) + v[4][k];
          } else {
#pragma unroll
            for (int p = 5; p < 8; ++p) { const unsigned a = rdr + 2304 * (p == 7 ? ns - 1 : p); v[p][0] = *reinterpret_cast<lp4>(a); v[p][1] = *reinterpret_cast<lp4>(a + 144); }
            SB();
#pragma unroll
            for (int k = 0; k < 2; ++k) {
              va[k] = v[0][k] + v[1][k];
#pragma unroll
              for (int p = 2; p < 8; ++p) { va[k] += (p < n1 ? 1.f : 0.f) * v[p][k]; vb[k] += (p >= n1 && p < ns ? 1.f : 0.f) * v[p][k]; }
            }
          }
#pragma unroll
          for (int k = 0; k < 2; ++k) {
            const u32x2 pk = s_pack(f32x4{tr32(va[k][0], vb[k][0]), tr32(va[k][1], vb[k][1]), tr32(va[k][2], vb[k][2]), tr32(va[k][3], vb[k][3])}, stg[2 * k], stg[2 * k + 1]);
            gs[2 * k] = pk[0]; gs[2 * k + 1] = pk[1];
          }
          st_ag(rw, dst, gs);
        }
        if ((blockIdx.x >> 3) == 16 && lane < TREP) st_ag(rw, WS_TM + 128 * lane + 16 * (blockIdx.x & 7), g_int(tstart, ((unsigned)__builtin_amdgcn_s_memrealtime() - tstart) & 4095u, tag));
        while ((unsigned)__builtin_amdgcn_s_memrealtime() - town < (unsigned)QT) __builtin_amdgcn_s_sleep(4);
        const int qo = WS_Q + (wi & 31) * DH * 8 + 16 * (lane < 48 ? lane : lane & 3);
        const int qo0 = lane >= 48 ? WS_TM + 128 * (h & (TREP - 1)) + 16 * (lane & 7) : qo;
        const u32x4 tg0 = lane < 48 ? stg : u32x4{tag.t[0], tag.t[1], tag.t[2], tag.t[3]};
        u32x4 g0, g1, g2, g3;
#pragma unroll 1
        for (int it = 0; it < SPIN_MAX; ++it) {
          FENCE();
          g0 = ld_ag(rw, qo0); g1 = ld_ag(rw, qo + NH * DH * 8);
          g2 = ld_ag(rw, qo + 768); g3 = ld_ag(rw, qo + NH * DH * 8 + 768);
          if (__all(((s_dif(g0, tg0) & (lane < 48 ? 15u : 1023u)) | ((s_dif(g1, stg) | s_dif(g2, stg) | s_dif(g3, stg)) & 15u)) == 0u)) break;
          __builtin_amdgcn_s_sleep(QBACK);
        }
        {
          const float d = fminf(fmaxf((float)((int)((g0[0] & ~1023u) - (tstart << 10)) >> 10), -300.f), 300.f);
          float sm = d + dpp<XOR1>(d); sm += dpp<XOR2>(sm); sm += dpp<XOR7>(sm);
          float mx = fminf(d, dpp<XOR1>(d)); mx = fminf(mx, dpp<XOR2>(mx)); mx = fminf(mx, dpp<XOR7>(mx));
          float lx = fmaxf(d, dpp<XOR1>(d)); lx = fmaxf(lx, dpp<XOR2>(lx)); lx = fmaxf(lx, dpp<XOR7>(lx));
          const float db = rintf(fmaxf(sm * (0.125f - CAL / 64.f) + mx * (CAL / 8.f), lx - CUP));
          if (lane == 48) { ctl[12] = (unsigned)(int)db; ctl[11] = (unsigned)(int)(lx - db); }
          float rd = (float)(g0[2] >> 10); rd += dpp<XOR1>(rd); rd += dpp<XOR2>(rd); rd += dpp<XOR7>(rd);
          if (lane == 48) ctl[14] = ((unsigned)fminf(fmaxf((rd * 0.125f - R0) * (RG / 8.f), 0.f), RMX) << 8);
        }
#pragma unroll
        for (int r = 0; r < 2; ++r) {
          const u32x4 ga = r ? g2 : g0, gb = r ? g3 : g1;
          float q[2][2];
#pragma unroll
          for (int k = 0; k < 2; ++k) {
            const float sa = s_scale(ga[2 * k], ga[2 * k + 1]), sb = s_scale(gb[2 * k], gb[2 * k + 1]);
            const unsigned a = wi < NH ? ga[2 * k] : ga[2 * k + 1], b = wi < NH ? gb[2 * k] : gb[2 * k + 1];
            q[0][k] = fmaf(s_hi(a), sa, s_hi(b) * sb); q[1][k] = fmaf(s_lo(a), sa, s_lo(b) * sb);
          }
          if (lane < 48) { qs[0][48 * r + lane] = cvtpk(q[0][0], q[0][1]); qs[1][48 * r + lane] = cvtpk(q[1][0], q[1][1]); }
        }
      }
      if (last) LDSINC(ctl[13]);
      LDSWAIT(ctl[13], 1, 8);
      tstart += __builtin_amdgcn_readfirstlane(ctl[12]);
    }
    if (NCK && w == 3) {
      unsigned xt = __builtin_amdgcn_readfirstlane(ctl[14]) >> 8, sl = 0;
      float pp = 0.f;
      const int ds = __builtin_amdgcn_readfirstlane(ctl[12]), dl = __builtin_amdgcn_readfirstlane(ctl[11]);
#pragma unroll 1
      for (int k = 0; k < NCK; ++k) {
        const int tl = ckt(k) + 1;
        const unsigned tp = tstart + TOFS + SPAN + CKO + sched(tl) + xt + 2 * sl;
        const int rem = min(max(NBUF - 1 + (1 << RSH) - tl, 0), 1 << RSH);
        while ((int)((unsigned)__builtin_amdgcn_s_memrealtime() - tp) < -((ds * rem) >> RSH)) __builtin_amdgcn_s_sleep(8);
        FENCE();
        const unsigned cr = ctl[16 + k];
        if ((h >> 3) == (sp & 3) && ((sp ^ (h >> 1)) & RPM) == 0 && lane < NREP) st_ag(rw, WS_CK + k * 8192 + lane * 1024 + (RPM == 0 ? sp * 8 + (h & 7) : RPM == 4 ? (sp & 3) * 8 + (h & 7) : (sp & 1) * 8 + (h & 7)) * 16, g_int(cr, 0u, tag));
        while ((int)((unsigned)__builtin_amdgcn_s_memrealtime() - tp) < CRD + ((dl * rem) >> RSH)) __builtin_amdgcn_s_sleep(4);
        u32x4 g0;
#pragma unroll 1
        for (int it = 0; it < 4; ++it) {
          FENCE();
          g0 = ld_ag(rw, WS_CK + k * 8192 + (h & (NREP - 1)) * 1024 + (lane & (RPM == 0 ? 63 : RPM == 4 ? 31 : 15)) * 16);
          if (__all(g_ok(g0, tag))) break;
        }
        const float p = PCAP - wave_sum(g_ok(g0, tag) ? (float)min(g0[0] >> 10, 4096u) : 0.f) * (1.f / 1024);
        const float df = k == 0 ? 0.1f * CG0 * (p - CZ0) : k < NCK - 1 ? 0.1f * CG1 * (p - CZ1)
                                 : fminf(fmaxf(0.1f * MA * (p - MZ), 0.1f * MB * (p - MY)), MC + 0.1f * MD * p);
        const unsigned d = (unsigned)fmaxf(fminf(df, 250.f), 0.f);
        xt = __builtin_amdgcn_readfirstlane(xt + d);
        if (k == 1) sl = __builtin_amdgcn_readfirstlane(p > SLZ && p > pp - SLD ? SLM : 0);
        if (k == 2 && p < SLY) sl = 0;
        pp = p;
        if (lane == 0) ctl[14] = (xt << 8) | sl;
      }
    }
    if (CPL && w == 2) {
      LDSWAIT(ctl[4], 1, 16);
      cv = ldb(rc, 16 * lane, cofs);
      asm volatile("" :: "v"(cv));
    }
    if (w == 1) {
      LDSWAIT(ctl[4], 8, 16);
      const float mi = mlw[0][lane & 15], li = mlw[1][lane & 15];
      float M = fmaxf(mi, dpp<XOR1>(mi)); M = fmaxf(M, dpp<XOR2>(M)); M = rnd22(fmaxf(M, dpp<XOR7>(M)));
      float L = li * __builtin_amdgcn_exp2f(mi - M);
      L += dpp<XOR1>(L); L += dpp<XOR2>(L); L += dpp<XOR7>(L);
      const u32x4 gp = g_pack(M, L, dpp<XOR8>(M), dpp<XOR8>(L), tag);
      if (lane == 0) st_ag(rw, WS_ML + (wi & 31) * MLH + (2 * mem + (wi >> 5)) * MLS, gp);
      if (blockIdx.x == 255) {
        const unsigned xw = __builtin_amdgcn_readfirstlane(ctl[14]), tml = tstart + (xw >> 8) + 10 * (xw & 255u);
        while ((unsigned)__builtin_amdgcn_s_memrealtime() - tml < (unsigned)sched(24) + TOFS + MLD + 30) __builtin_amdgcn_s_sleep(8);
        bool ok = false;
#pragma unroll 1
        for (int it = 0; it < SPIN_MAX && !ok; ++it) {
          FENCE();
          ok = true;
#pragma unroll
          for (int i = 0; i < 4; ++i) ok &= g_ok(ld_ag(rw, WS_ML + (8 * i + (lane >> 3)) * MLH + (lane & 7) * MLS), tag);
          ok = __all(ok);
          if (!ok) __builtin_amdgcn_s_sleep(32);
        }
        if (ok && lane == 0) __hip_atomic_store(reinterpret_cast<u64*>(ws + WS_EPOCH), *reinterpret_cast<const u64*>(&ctl[0]), __ATOMIC_RELAXED, __HIP_MEMORY_SCOPE_AGENT);
      }
      if (proj) {
        ROLES();
        const unsigned xw = __builtin_amdgcn_readfirstlane(ctl[14]), tml = tstart + (xw >> 8) + 10 * (xw & 255u);
        while ((unsigned)__builtin_amdgcn_s_memrealtime() - tml < (unsigned)sched(24) + TOFS + MLD + 30) __builtin_amdgcn_s_sleep(8);
        u32x4 g0;
#pragma unroll 1
        for (int it = 0; it < SPIN_MAX; ++it) {
          FENCE();
          g0 = ld_ag(rw, WS_ML + grp * MLH + (2 * (lane & 3) + ((lane >> 2) & 1)) * MLS);
          if (__all(g_ok(g0, tag))) break;
          __builtin_amdgcn_s_sleep(12);
        }
        const f32x4 a0 = g_val(g0, tag);
        const float mq = (lane & 8) ? a0[2] : a0[0], lq = (lane & 8) ? a0[3] : a0[1];
        float Mx = fmaxf(mq, dpp<XOR1>(mq)); Mx = fmaxf(Mx, dpp<XOR2>(Mx));
        const float e0 = __builtin_amdgcn_exp2f(mq - Mx);
        float lt = e0 * lq; lt += dpp<XOR1>(lt); lt += dpp<XOR2>(lt);
        mlw[2][lane & 15] = e0 / lt;
        LDSINC(ctl[9]);
      }
    }
    ROLES();
    if (w == 0 && ffn) {
      while ((int)((unsigned)__builtin_amdgcn_s_memrealtime() - tstart) < FGT + PHS) __builtin_amdgcn_s_sleep(8);
      const int go = WS_GU + 96 * grp * 8 + 16 * (lane < 48 ? lane : lane & 3);
      const u32x4 stg = s_tag(__builtin_amdgcn_readfirstlane(ctl[0]), lane);
      u32x4 g[4];
      const u32x4 xv = ldb(mkrs(x, grp == 0 ? NB * DM * 2 : 0), ((lane >> 4) * DM + 128 * cb + 8 * (lane & 15)) * 2, 0);
#pragma unroll 1
      for (int it = 0; it < SPIN_MAX; ++it) {
        FENCE();
#pragma unroll
        for (int i = 0; i < 4; ++i) g[i] = ld_ag(rw, go + i * FF * 8);
        if (__all(((s_dif(g[0], stg) | s_dif(g[1], stg) | s_dif(g[2], stg) | s_dif(g[3], stg)) & 15u) == 0u)) break;
        __builtin_amdgcn_s_sleep(32);
      }
      float hv[2][NB];
#pragma unroll
      for (int k = 0; k < 2; ++k) {
        float sc4[4];
#pragma unroll
        for (int i = 0; i < 4; ++i) sc4[i] = s_scale(g[i][2 * k], g[i][2 * k + 1]);
#pragma unroll
        for (int bb = 0; bb < NB; ++bb) {
          float v[4];
#pragma unroll
          for (int i = 0; i < 4; ++i) v[i] = bb < 2 ? s_hi(g[i][2 * k + bb]) : s_lo(g[i][2 * k + bb - 2]);
          const float gg = rbf(fmaf(v[0], sc4[0], v[2] * sc4[2])), up = rbf(fmaf(v[1], sc4[1], v[3] * sc4[3]));
          hv[k][bb] = rbf(gg / (1.f + __expf(-gg))) * up;
        }
      }
#pragma unroll
      for (int bb = 0; bb < NB; ++bb) oA[bb][lane] = cvtpk(hv[0][bb], hv[1][bb]);
      reinterpret_cast<u32x4*>(&red[0][0][0])[lane] = xv;
    }
    if (w < 3 && (proj || ffn)) {
      LDSWAIT(ctl[8], 8, 2);
      tstart = __builtin_amdgcn_readfirstlane(clk[1]) + sched(31) - PHS;
      while ((int)((unsigned)__builtin_amdgcn_s_memrealtime() - tstart) < (proj ? WT + ((grp * WPH) >> 5) : WTF + ((grp * WPF) >> 4))) __builtin_amdgcn_s_sleep(1);
      const rsrc_t ru = mkrs(proj ? wo + (size_t)(grp * DH + 64 * w) * DM : w2 + (size_t)(96 * grp + 32 * w) * DM, (proj ? 64 : 32) * DM * 2);
      const int vo = 8 * (lane >> 4) * (DM * 2) + 256 * cb + 16 * (lane & 15);
      u32x4 wl[16];
#pragma unroll
      for (int e = 0; e < 8; ++e) wl[e] = ldb_nt(ru, vo, e * (DM * 2));
      SB();
      if (proj) {
        while ((int)((unsigned)__builtin_amdgcn_s_memrealtime() - tstart) < WB + ((grp * WPB) >> 5)) __builtin_amdgcn_s_sleep(1);
        SB();
#pragma unroll
        for (int e = 0; e < 8; ++e) wl[8 + e] = ldb_nt(ru, vo, (32 + e) * (DM * 2));
        SB();
#pragma unroll
        for (int ec = 0; ec < 8; ++ec) wf[w][ec][lane] = bfrag(&wl[0], ec);
        LDSINC(ctl[15]);
        SB();
        u32x4 bw[8];
#pragma unroll
        for (int p = 0; p < 3; ++p)
#pragma unroll
          for (int ec = 0; ec < 8; ++ec) { bw[ec][p] = bfrag(&wl[8], ec)[p]; asm volatile("" : "+v"(bw[ec][p])); }
        SB();
#pragma unroll
        for (int ec = 0; ec < 8; ++ec) { bw[ec][3] = bfrag(&wl[8], ec)[3]; wf[3 + w][ec][lane] = bw[ec]; }
      } else {
#pragma unroll
        for (int ec = 0; ec < 8; ++ec) wf[w][ec][lane] = bfrag(&wl[0], ec);
        LDSINC(ctl[15]);
      }
    }
    LDSINC(ctl[6]);
    if (proj && w < 3) BAR();
  }
}

extern "C" void kernel_launch(void* const* d_in, const int* in_sizes, int n_in, void* d_out, int out_size, void* d_ws,
                              size_t ws_size, hipStream_t stream) {
  fused_k<<<256, 768, 0, stream>>>((const bf16*)d_in[0], (const bf16*)d_in[1], (const bf16*)d_in[2], (const bf16*)d_in[3],
                                   (const bf16*)d_in[4], (const bf16*)d_in[6], (const bf16*)d_in[7], (const bf16*)d_in[8],
                                   (unsigned char*)d_ws, (bf16*)d_out);
}
```

```cpp
#include <hip/hip_runtime.h>
#include <hip/hip_bf16.h>
#include <cstdint>

using bf16 = __hip_bfloat16;
using u32x2 = __attribute__((ext_vector_type(2))) unsigned;
using u32x4 = __attribute__((ext_vector_type(4))) unsigned;
using f32x2 = __attribute__((ext_vector_type(2))) float;
using f32x4 = __attribute__((ext_vector_type(4))) float;
using bf16x2 = __attribute__((ext_vector_type(2))) __bf16;
using bf16x8 = __attribute__((ext_vector_type(8))) __bf16;
typedef unsigned long long u64;

constexpr int NB = 4, DM = 640, NH = 32, DH = 192, SK = 8192, WIN = 2048, FF = 1536;
constexpr float LOG2E = 1.4426950408889634f;
constexpr float SCALE = 0.07216878364870322f;
constexpr int WINL = 4;
constexpr int WINV = 12;
constexpr int TSW = 24;
constexpr int QT = 570;
constexpr int QBACK = 16;
constexpr int TAU = 86;
constexpr int TK = 11;
constexpr int TAU2 = 160;
constexpr int PR1 = 60;
constexpr int PR2 = 150;
constexpr int PR3 = 300;
constexpr int PRS = 3;
constexpr int CPE = 1;
constexpr int CPL = 1;
constexpr int CODEB = 29424;
constexpr int PT1 = 195;
constexpr int ET = 5;
constexpr int SPAN = 80;
constexpr int TOFS = 116;
constexpr int RING = 9;
constexpr int WTRIG = 31;
constexpr int LATE = 100;
constexpr int WINB = 8;
constexpr int MLD = 300;
constexpr int PSL1 = 6;
constexpr int RSH = 3;
constexpr int CAL = 5;
constexpr int LG = 4;
constexpr int WT = 78;
constexpr int FW1 = 1;
constexpr int WTF = 122;
constexpr int NCK = 3;
constexpr int CK0 = 7;
constexpr int CKL = 18;
constexpr int CKS = 6;
constexpr int CG0 = 36;
constexpr int CZ0 = 30;
constexpr int CG1 = 30;
constexpr int CZ1 = 20;
constexpr int MA = 22;
constexpr int MZ = 18;
constexpr int MB = 200;
constexpr int MY = 27;
constexpr int MC = 60;
constexpr int MD = 10;
constexpr int CRD = 110;
constexpr int YP = 8;
constexpr int YL = 256;
constexpr int YLO = 99u;
constexpr int YHI = 139u;
constexpr int WB = 144;
constexpr int WPH = 44;
constexpr int WPB = 50;
constexpr int PD = 52;
constexpr int CKO = 35;
constexpr int RG = 8;
constexpr int R0 = 415;
constexpr int RMX = 120;
constexpr int LGN = 12;
constexpr int L0 = 75;
constexpr int NREP = 2;
constexpr int MLS = 16;
constexpr int MLH = 256;
constexpr int FGT = 1010;
constexpr int TREP = 2;
constexpr int TCK = 1;
constexpr int TYM = 15;
constexpr int TWM = 15;
constexpr int TOM = 7;
constexpr int RPM = 4;
constexpr int CP0 = 100;
constexpr int CP1 = 60;
constexpr int CP2 = 45;
constexpr int CUP = 65;
constexpr int SLZ = 29;
constexpr int SLD = 5;
constexpr int SLM = 4;
constexpr int SLY = 18;
constexpr int TGT = 28;
constexpr int TGG = 10;
constexpr int NPH = 20;
constexpr int WPF = 22;
constexpr int SOFS = 30;
constexpr int UID = 0;
constexpr int PF = 4;
constexpr int ALT = 0;
constexpr int AO = 0;
constexpr int ZE = 0;
constexpr int TOL = 0;
constexpr int RB = 0;
constexpr int HQX = 5;
constexpr int sched(int t) { return t <= TK ? TAU * t : TAU * TK + ((TAU2 * (t - TK)) >> 1) + (t >= TGT ? TGG : 0); }
constexpr int viss0(int v) { return v <= 2 * (RING - 1 - LG) + 1 ? ((v & 1) ? 16 + RING + (v >> 1) : 0) : 16 + v + LG; }
constexpr int viss(int v) { return viss0(v) < 32 ? viss0(v) : 0; }
constexpr int PCAP = 150;
constexpr int ckt(int k) { return k == NCK - 1 ? CKL : CK0 + k * CKS; }
constexpr int ckof(int t) { for (int k = 0; k < NCK; ++k) if (t == ckt(k) || t == ckt(k) + 1) return k; return -1; }
constexpr int NBUF = RING;
constexpr int NT = 16;
constexpr int TSTR = 4 * 8 * 8 * DH * 2;
constexpr int BSTR = 2 * NH * SK * DH * 2;

constexpr int WS_EPOCH = 0;
constexpr int WS_Q = 512;
constexpr int WS_GU = WS_Q + 2 * NH * DH * 8;
constexpr int WS_ML = WS_GU + 2 * 2 * FF * 8;
constexpr int WS_TM = WS_ML + NH * MLH;
constexpr int WS_CK = WS_TM + 8 * 128;
constexpr int WS_O = WS_CK + 4 * 8 * 1024;
constexpr int NY = NH + 16;
constexpr int WS_Y = (WS_O + NH * NB * 4 * DH * 4 + 65535) & ~65535;
constexpr int WS_S = WS_Y + (NB * DM / YP) * YL;
constexpr int WS_END = WS_S + NY * NB * DM * 8;

__device__ __forceinline__ float dot2(unsigned k, unsigned q, float acc) {
  return __builtin_amdgcn_fdot2_f32_bf16(__builtin_bit_cast(bf16x2, k), __builtin_bit_cast(bf16x2, q), acc, false);
}
__device__ __forceinline__ float bflo(unsigned v) { return __builtin_bit_cast(float, v << 16); }
__device__ __forceinline__ float bfhi(unsigned v) { return __builtin_bit_cast(float, v & 0xffff0000u); }
template <int CTRL> __device__ __forceinline__ float dpp(float x) {
  return __builtin_bit_cast(float, __builtin_amdgcn_mov_dpp(__builtin_bit_cast(int, x), CTRL, 0xf, 0xf, true));
}
constexpr int XOR1 = 0xB1, XOR2 = 0x4E, XOR7 = 0x141, XOR8 = 0x128;
__device__ __forceinline__ float x16sum(float x) {
  auto s = __builtin_amdgcn_permlane16_swap(__float_as_uint(x), __float_as_uint(x), false, false);
  return __uint_as_float(s[0]) + __uint_as_float(s[1]);
}
__device__ __forceinline__ float x32sum(float x) {
  auto t = __builtin_amdgcn_permlane32_swap(__float_as_uint(x), __float_as_uint(x), false, false);
  return __uint_as_float(t[0]) + __uint_as_float(t[1]);
}
__device__ __forceinline__ float x16max(float x) {
  auto s = __builtin_amdgcn_permlane16_swap(__float_as_uint(x), __float_as_uint(x), false, false);
  return fmaxf(__uint_as_float(s[0]), __uint_as_float(s[1]));
}
__device__ __forceinline__ float x32max(float x) {
  auto t = __builtin_amdgcn_permlane32_swap(__float_as_uint(x), __float_as_uint(x), false, false);
  return fmaxf(__uint_as_float(t[0]), __uint_as_float(t[1]));
}
__device__ __forceinline__ float sum_kk(float x) { x += dpp<XOR8>(x); x = x16sum(x); return x32sum(x); }
__device__ __forceinline__ float wave_sum(float x) {
  x += dpp<XOR1>(x); x += dpp<XOR2>(x); x += dpp<XOR7>(x); return sum_kk(x);
}
__device__ __forceinline__ float tr32(float a, float b) {
  auto t = __builtin_amdgcn_permlane32_swap(__float_as_uint(a), __float_as_uint(b), false, false);
  return __uint_as_float(t[0]) + __uint_as_float(t[1]);
}
__device__ __forceinline__ float tr16(float a, float b) {
  auto t = __builtin_amdgcn_permlane16_swap(__float_as_uint(a), __float_as_uint(b), false, false);
  return __uint_as_float(t[0]) + __uint_as_float(t[1]);
}
__device__ __forceinline__ unsigned cvtpk(float lo, float hi) {
  return __builtin_bit_cast(unsigned, __builtin_convertvector(f32x2{lo, hi}, bf16x2));
}
__device__ __forceinline__ float rbf(float v) { return bflo(cvtpk(v, 0.f)); }
__device__ __forceinline__ f32x4 mfma(u32x4 a, u32x4 b, f32x4 c) {
  return __builtin_amdgcn_mfma_f32_16x16x32_bf16(__builtin_bit_cast(bf16x8, a), __builtin_bit_cast(bf16x8, b), c, 0, 0, 0);
}
__device__ __forceinline__ u32x4 bfrag(const u32x4* r, int ec) {
  u32x4 f;
#pragma unroll
  for (int p = 0; p < 4; ++p) f[p] = __builtin_amdgcn_perm(r[2 * p + 1][ec >> 1], r[2 * p][ec >> 1], (ec & 1) ? 0x07060302u : 0x05040100u);
  return f;
}
typedef __amdgpu_buffer_rsrc_t rsrc_t;
__device__ __forceinline__ rsrc_t mkrs(const void* p, unsigned n) { return __builtin_amdgcn_make_buffer_rsrc(const_cast<void*>(p), (short)0, (int)n, 0x00020000); }
constexpr int AUX_NT = 2, AUX_SC1 = 16;
__device__ __forceinline__ u32x4 ldb(rsrc_t rs, int voff, int soff) { return __builtin_amdgcn_raw_buffer_load_b128(rs, voff, soff, 0); }
__device__ __forceinline__ u32x4 ldb_nt(rsrc_t rs, int voff, int soff) { return __builtin_amdgcn_raw_buffer_load_b128(rs, voff, soff, AUX_NT); }
__device__ __forceinline__ u32x4 ld_ag(rsrc_t rs, int voff) { return __builtin_amdgcn_raw_buffer_load_b128(rs, voff, 0, AUX_SC1); }
__device__ __forceinline__ void st_ag(rsrc_t rs, int voff, u32x4 v) { __builtin_amdgcn_raw_buffer_store_b128(v, rs, voff, 0, AUX_SC1); }
struct Tag { unsigned t[4]; };
__device__ __forceinline__ Tag mktag(unsigned e) { return Tag{{e & 1023u, (e + 2 * (e >> 10)) & 1023u, (e + 0x156u) & 1023u, (e + 2 * (e >> 19) + 0x2a8u) & 1023u}}; }
__device__ __forceinline__ unsigned r22(float v) { return (__float_as_uint(v) + 0x200u) & ~1023u; }
__device__ __forceinline__ float rnd22(float v) { return __uint_as_float(r22(v)); }
__device__ __forceinline__ u32x4 g_pack(float a, float b, float c, float d, Tag t) {
  return u32x4{r22(a) | t.t[0], r22(b) | t.t[1], r22(c) | t.t[2], r22(d) | t.t[3]};
}
__device__ __forceinline__ u32x4 g_int(unsigned a, unsigned c, Tag t) { return u32x4{(a << 10) | t.t[0], t.t[1], (c << 10) | t.t[2], t.t[3]}; }
__device__ __forceinline__ bool g_ok(u32x4 g, Tag t) { return (((g[0] ^ t.t[0]) | (g[1] ^ t.t[1]) | (g[2] ^ t.t[2]) | (g[3] ^ t.t[3])) & 1023u) == 0u; }
__device__ __forceinline__ f32x4 g_val(u32x4 g, Tag t) {
  return f32x4{__uint_as_float(g[0] ^ t.t[0]), __uint_as_float(g[1] ^ t.t[1]), __uint_as_float(g[2] ^ t.t[2]), __uint_as_float(g[3] ^ t.t[3])};
}
__device__ __forceinline__ u32x4 s_tag(unsigned e, int lane) {
  u32x4 t;
#pragma unroll
  for (int i = 0; i < 4; ++i) t[i] = (e + 2 * (e >> (4 + 7 * i + 2 * (lane & 3)))) & 15u;
  return t;
}
__device__ __forceinline__ u32x2 s_pack(f32x4 v, unsigned t0, unsigned t1) {
  const unsigned eb = max(max(__float_as_uint(v[0]) << 1, __float_as_uint(v[1]) << 1), max(__float_as_uint(v[2]) << 1, __float_as_uint(v[3]) << 1)) >> 24;
  unsigned n[4];
#pragma unroll
  for (int i = 0; i < 4; ++i) n[i] = (unsigned)min((int)rintf(__builtin_amdgcn_ldexpf(v[i], 137 - (int)eb)), 2047);
  return u32x2{(n[0] << 20) | ((n[2] & 4095u) << 8) | ((eb & 15u) << 4) | t0, (n[1] << 20) | ((n[3] & 4095u) << 8) | (eb & 240u) | t1};
}
__device__ __forceinline__ unsigned s_dif(u32x4 g, u32x4 t) { return (g[0] ^ t[0]) | (g[1] ^ t[1]) | (g[2] ^ t[2]) | (g[3] ^ t[3]); }
__device__ __forceinline__ float s_scale(unsigned a, unsigned b) { return __uint_as_float(((a & 240u) << 19) | ((b & 240u) << 23)) * (1.f / 1024); }
__device__ __forceinline__ float s_hi(unsigned a) { return (float)((int)a >> 20); }
__device__ __forceinline__ float s_lo(unsigned a) { return (float)((int)(a << 12) >> 20); }
constexpr int SPIN_MAX = 1 << 18;
#define SB() __builtin_amdgcn_sched_barrier(0)
#define FENCE() asm volatile("" ::: "memory")
#define BAR() asm volatile("s_waitcnt lgkmcnt(0)\n\ts_barrier" ::: "memory")
#define VMW(n) asm volatile("s_waitcnt vmcnt(%0)" :: "n"(n) : "memory")
template <int SL, int FW> __device__ __forceinline__ void poll4(f32x4 (&r)[4], u32x4 (&p)[12], const int (&f)[4], rsrc_t rs, Tag tag) {
  unsigned t, u, z, cnt;
  asm volatile(
      "s_mov_b32 %[cnt], %[mx]\n\t"
      ".if %[fw]\n\t"
      "buffer_load_dwordx4 v[64:67], %[f0], %[rs], 0 offen sc1\n\tbuffer_load_dwordx4 v[68:71], %[f1], %[rs], 0 offen sc1\n\tbuffer_load_dwordx4 v[72:75], %[f2], %[rs], 0 offen sc1\n\tbuffer_load_dwordx4 v[76:79], %[f3], %[rs], 0 offen sc1\n\t"
      "s_waitcnt vmcnt(0)\n\tv_xor_b32 v112, %[t0], v64\n\tv_xor_b32 v113, %[t1], v65\n\tv_xor_b32 v114, %[t2], v66\n\tv_xor_b32 v115, %[t3], v67\n\tv_xor_b32 v116, %[t0], v68\n\tv_xor_b32 v117, %[t1], v69\n\tv_xor_b32 v118, %[t2], v70\n\tv_xor_b32 v119, %[t3], v71\n\tv_xor_b32 v120, %[t0], v72\n\tv_xor_b32 v121, %[t1], v73\n\tv_xor_b32 v122, %[t2], v74\n\tv_xor_b32 v123, %[t3], v75\n\tv_xor_b32 v124, %[t0], v76\n\tv_xor_b32 v125, %[t1], v77\n\tv_xor_b32 v126, %[t2], v78\n\tv_xor_b32 v127, %[t3], v79\n\tv_or3_b32 %[t], v112, v113, v114\n\tv_or3_b32 %[u], v115, v116, v117\n\tv_or3_b32 %[z], v118, v119, v120\n\tv_or3_b32 %[t], %[t], v121, v122\n\tv_or3_b32 %[u], %[u], v123, v124\n\tv_or3_b32 %[z], %[z], v125, v126\n\tv_or3_b32 %[t], %[t], %[u], %[z]\n\tv_or_b32 %[t], %[t], v127\n\tv_lshlrev_b32 %[t], 22, %[t]\n\tv_cmp_ne_u32 vcc, 0, %[t]\n\ts_mov_b64 exec, vcc\n\ts_cbranch_execz 2%=f\n\t"
      ".endif\n\t"
      "buffer_load_dwordx4 v[64:67], %[f0], %[rs], 0 offen sc1\n\tbuffer_load_dwordx4 v[68:71], %[f1], %[rs], 0 offen sc1\n\tbuffer_load_dwordx4 v[72:75], %[f2], %[rs], 0 offen sc1\n\tbuffer_load_dwordx4 v[76:79], %[f3], %[rs], 0 offen sc1\n\ts_sleep %[sl]\n\t"
      "buffer_load_dwordx4 v[80:83], %[f0], %[rs], 0 offen sc1\n\tbuffer_load_dwordx4 v[84:87], %[f1], %[rs], 0 offen sc1\n\tbuffer_load_dwordx4 v[88:91], %[f2], %[rs], 0 offen sc1\n\tbuffer_load_dwordx4 v[92:95], %[f3], %[rs], 0 offen sc1\n\ts_sleep %[sl]\n\t"
      "1%=:\n\t"
      "buffer_load_dwordx4 v[96:99], %[f0], %[rs], 0 offen sc1\n\tbuffer_load_dwordx4 v[100:103], %[f1], %[rs], 0 offen sc1\n\tbuffer_load_dwordx4 v[104:107], %[f2], %[rs], 0 offen sc1\n\tbuffer_load_dwordx4 v[108:111], %[f3], %[rs], 0 offen sc1\n\t"
      "s_waitcnt vmcnt(8)\n\tv_xor_b32 v112, %[t0], v64\n\tv_xor_b32 v113, %[t1], v65\n\tv_xor_b32 v114, %[t2], v66\n\tv_xor_b32 v115, %[t3], v67\n\tv_xor_b32 v116, %[t0], v68\n\tv_xor_b32 v117, %[t1], v69\n\tv_xor_b32 v118, %[t2], v70\n\tv_xor_b32 v119, %[t3], v71\n\tv_xor_b32 v120, %[t0], v72\n\tv_xor_b32 v121, %[t1], v73\n\tv_xor_b32 v122, %[t2], v74\n\tv_xor_b32 v123, %[t3], v75\n\tv_xor_b32 v124, %[t0], v76\n\tv_xor_b32 v125, %[t1], v77\n\tv_xor_b32 v126, %[t2], v78\n\tv_xor_b32 v127, %[t3], v79\n\tv_or3_b32 %[t], v112, v113, v114\n\tv_or3_b32 %[u], v115, v116, v117\n\tv_or3_b32 %[z], v118, v119, v120\n\tv_or3_b32 %[t], %[t], v121, v122\n\tv_or3_b32 %[u], %[u], v123, v124\n\tv_or3_b32 %[z], %[z], v125, v126\n\tv_or3_b32 %[t], %[t], %[u], %[z]\n\tv_or_b32 %[t], %[t], v127\n\tv_lshlrev_b32 %[t], 22, %[t]\n\tv_cmp_ne_u32 vcc, 0, %[t]\n\ts_mov_b64 exec, vcc\n\ts_cbranch_execz 2%=f\n\t"
      "buffer_load_dwordx4 v[64:67], %[f0], %[rs], 0 offen sc1\n\tbuffer_load_dwordx4 v[68:71], %[f1], %[rs], 0 offen sc1\n\tbuffer_load_dwordx4 v[72:75], %[f2], %[rs], 0 offen sc1\n\tbuffer_load_dwordx4 v[76:79], %[f3], %[rs], 0 offen sc1\n\t"
      "s_waitcnt vmcnt(8)\n\tv_xor_b32 v112, %[t0], v80\n\tv_xor_b32 v113, %[t1], v81\n\tv_xor_b32 v114, %[t2], v82\n\tv_xor_b32 v115, %[t3], v83\n\tv_xor_b32 v116, %[t0], v84\n\tv_xor_b32 v117, %[t1], v85\n\tv_xor_b32 v118, %[t2], v86\n\tv_xor_b32 v119, %[t3], v87\n\tv_xor_b32 v120, %[t0], v88\n\tv_xor_b32 v121, %[t1], v89\n\tv_xor_b32 v122, %[t2], v90\n\tv_xor_b32 v123, %[t3], v91\n\tv_xor_b32 v124, %[t0], v92\n\tv_xor_b32 v125, %[t1], v93\n\tv_xor_b32 v126, %[t2], v94\n\tv_xor_b32 v127, %[t3], v95\n\tv_or3_b32 %[t], v112, v113, v114\n\tv_or3_b32 %[u], v115, v116, v117\n\tv_or3_b32 %[z], v118, v119, v120\n\tv_or3_b32 %[t], %[t], v121, v122\n\tv_or3_b32 %[u], %[u], v123, v124\n\tv_or3_b32 %[z], %[z], v125, v126\n\tv_or3_b32 %[t], %[t], %[u], %[z]\n\tv_or_b32 %[t], %[t], v127\n\tv_lshlrev_b32 %[t], 22, %[t]\n\tv_cmp_ne_u32 vcc, 0, %[t]\n\ts_mov_b64 exec, vcc\n\ts_cbranch_execz 2%=f\n\t"
      "buffer_load_dwordx4 v[80:83], %[f0], %[rs], 0 offen sc1\n\tbuffer_load_dwordx4 v[84:87], %[f1], %[rs], 0 offen sc1\n\tbuffer_load_dwordx4 v[88:91], %[f2], %[rs], 0 offen sc1\n\tbuffer_load_dwordx4 v[92:95], %[f3], %[rs], 0 offen sc1\n\t"
      "s_waitcnt vmcnt(8)\n\tv_xor_b32 v112, %[t0], v96\n\tv_xor_b32 v113, %[t1], v97\n\tv_xor_b32 v114, %[t2], v98\n\tv_xor_b32 v115, %[t3], v99\n\tv_xor_b32 v116, %[t0], v100\n\tv_xor_b32 v117, %[t1], v101\n\tv_xor_b32 v118, %[t2], v102\n\tv_xor_b32 v119, %[t3], v103\n\tv_xor_b32 v120, %[t0], v104\n\tv_xor_b32 v121, %[t1], v105\n\tv_xor_b32 v122, %[t2], v106\n\tv_xor_b32 v123, %[t3], v107\n\tv_xor_b32 v124, %[t0], v108\n\tv_xor_b32 v125, %[t1], v109\n\tv_xor_b32 v126, %[t2], v110\n\tv_xor_b32 v127, %[t3], v111\n\tv_or3_b32 %[t], v112, v113, v114\n\tv_or3_b32 %[u], v115, v116, v117\n\tv_or3_b32 %[z], v118, v119, v120\n\tv_or3_b32 %[t], %[t], v121, v122\n\tv_or3_b32 %[u], %[u], v123, v124\n\tv_or3_b32 %[z], %[z], v125, v126\n\tv_or3_b32 %[t], %[t], %[u], %[z]\n\tv_or_b32 %[t], %[t], v127\n\tv_lshlrev_b32 %[t], 22, %[t]\n\tv_cmp_ne_u32 vcc, 0, %[t]\n\ts_mov_b64 exec, vcc\n\ts_cbranch_execz 2%=f\n\t"
      "s_sub_u32 %[cnt], %[cnt], 1\n\ts_cmp_lg_u32 %[cnt], 0\n\ts_cbranch_scc1 1%=b\n2%=:\n\ts_mov_b64 exec, -1"
      : "=&{v[112:115]}"(r[0]), "=&{v[116:119]}"(r[1]), "=&{v[120:123]}"(r[2]), "=&{v[124:127]}"(r[3]),
        "=&{v[64:67]}"(p[0]), "=&{v[68:71]}"(p[1]), "=&{v[72:75]}"(p[2]), "=&{v[76:79]}"(p[3]), "=&{v[80:83]}"(p[4]), "=&{v[84:87]}"(p[5]), "=&{v[88:91]}"(p[6]), "=&{v[92:95]}"(p[7]), "=&{v[96:99]}"(p[8]), "=&{v[100:103]}"(p[9]), "=&{v[104:107]}"(p[10]), "=&{v[108:111]}"(p[11]),
        [t] "=&v"(t), [u] "=&v"(u), [z] "=&v"(z), [cnt] "=&s"(cnt)
      : [f0] "v"(f[0]), [f1] "v"(f[1]), [f2] "v"(f[2]), [f3] "v"(f[3]), [rs] "s"(rs), [t0] "s"(tag.t[0]), [t1] "s"(tag.t[1]), [t2] "s"(tag.t[2]), [t3] "s"(tag.t[3]), [mx] "n"(SPIN_MAX), [sl] "n"(SL), [fw] "n"(FW)
      : "vcc", "scc", "memory");
}
__device__ __forceinline__ void poll4_keep(u32x4 (&p)[12]) { asm volatile("" :: "{v[64:67]}"(p[0]), "{v[68:71]}"(p[1]), "{v[72:75]}"(p[2]), "{v[76:79]}"(p[3]), "{v[80:83]}"(p[4]), "{v[84:87]}"(p[5]), "{v[88:91]}"(p[6]), "{v[92:95]}"(p[7]), "{v[96:99]}"(p[8]), "{v[100:103]}"(p[9]), "{v[104:107]}"(p[10]), "{v[108:111]}"(p[11])); }
#define LDSWAITV(fs_, word, n, slp) do { unsigned fv_; \
    asm volatile("1%=:\n\tds_read_b32 %0, %2\n\ts_waitcnt lgkmcnt(0)\n\tv_readfirstlane_b32 %1, %0\n\ts_cmp_lt_u32 %1, %3\n\ts_cbranch_scc0 2%=f\n\ts_sleep %4\n\ts_branch 1%=b\n2%=:" \
                 : "=&v"(fv_), "=&s"(fs_) : "v"((unsigned)(uintptr_t)&(word)), "n"(n), "n"(slp) : "scc", "memory"); } while (0)
#define LDSINC(word) asm volatile("s_mov_b64 exec, 1\n\tds_add_u32 %0, %1\n\ts_mov_b64 exec, -1" :: "v"((unsigned)(uintptr_t)&(word)), "v"(1u) : "memory")
#define LDSWAIT(word, n, slp) do { unsigned fs0_; LDSWAITV(fs0_, word, n, slp); } while (0)

#define LAND(n) asm volatile(".Lland%0_" #n ":\n\ts_cbranch_execz .Lback%0" :: "n"(UID))
constexpr int ASTR = 132;

__global__ void __attribute__((aligned(32768))) __launch_bounds__(768, 3)
fused_k(const bf16* __restrict__ x, const bf16* __restrict__ kc, const bf16* __restrict__ vc, const bf16* __restrict__ wq,
        const bf16* __restrict__ wo, const bf16* __restrict__ w1, const bf16* __restrict__ w3, const bf16* __restrict__ w2,
        unsigned char* __restrict__ ws, bf16* __restrict__ out) {
  __shared__ __attribute__((aligned(16))) f32x4 red[16][8][9];
  __shared__ __attribute__((aligned(16))) float stat[NB][2];
  __shared__ __attribute__((aligned(16))) unsigned qs[2][DH / 2];
  __shared__ __attribute__((aligned(16))) unsigned clk[8];
  __shared__ __attribute__((aligned(16))) unsigned ctl[20];
  __shared__ __attribute__((aligned(16))) float mrg[2][8][DH];
  __shared__ __attribute__((aligned(16))) float mlw[3][16];
  __shared__ __attribute__((aligned(16))) unsigned oA[NB][ASTR];
  __shared__ __attribute__((aligned(16))) u32x4 wf[6][8][64];
  asm volatile("s_cmp_gt_u32 %1, 255\n\ts_cbranch_scc1 .Lback%0\n\ts_mov_b64 exec, 0\n\ts_cmp_lt_u32 %1, 64\n\ts_cbranch_scc1 .Lland%0_0\n\t"
               "s_cmpk_lt_u32 %1, 128\n\ts_cbranch_scc1 .Lland%0_1\n\ts_cmpk_lt_u32 %1, 192\n\ts_cbranch_scc1 .Lland%0_2\n\ts_branch .Lland%0_3\n"
               ".Lback%0:\n\ts_mov_b64 exec, -1" :: "n"(UID), "s"(__builtin_amdgcn_readfirstlane(threadIdx.x)) : "scc");
  SB();
  u64 pc0;
  asm volatile("s_getpc_b64 %0" : "=s"(pc0));
  asm volatile("" :: "s"(x), "s"(kc), "s"(vc), "s"(wq), "s"(wo), "s"(w1), "s"(w3), "s"(w2), "s"(ws), "s"(out));
#define HOF(b) (ALT == 1 ? ((b) & 24) | (((b) & 3) << 1) | (((b) >> 2) & 1) : ALT == 2 ? ((b) & 25) | (((b) & 2) << 1) | (((b) >> 1) & 2) : ALT == 3 ? ((b) & 31) ^ 4 : (b) & 31)
#define ROF(h) (RB == 1 ? ((h) >= 18 && (h) < 22 ? (h) ^ 6 : (h) == 27 || (h) == 30 ? (h) ^ 5 : (h)) : RB == 2 ? ((h) >= 16 && (h) < 24 ? (h) ^ 4 : (h)) : (h))
  const int h = HOF(blockIdx.x), sp = blockIdx.x >> 5, pr = sp & 1, mem = sp >> 1;
  const int wi = AO ? 2 * h + pr : NH * pr + h;
#define PHS (AO == 2 ? (wi * SPAN) >> 6 : (h * SPAN) >> 5)
  const int tid = threadIdx.x, lane = tid & 63, wave = __builtin_amdgcn_readfirstlane(tid >> 6);
  const rsrc_t rw = mkrs(ws, WS_END);
  Tag tag;
  const bool proj = ROF(h) < NPH, ffn = ROF(h) >= NPH && ROF(h) < NPH + 10;
#define ROLES() int bx_ = blockIdx.x; asm volatile("" : "+s"(bx_)); \
  const int ru8 = 8 * (ROF(HOF(bx_)) - (proj ? 0 : NPH)) + (bx_ >> 5), grp = (ru8 * 205) >> 10, cb = ru8 - 5 * grp; (void)cb
  if (__builtin_expect(wave >= 4, 1)) {
    const int w = wave - 4, kk = lane >> 3, sub = lane & 7;
    const size_t wbase = ((size_t)wi * SK + (SK - WIN)) * DH;
    const rsrc_t rk = mkrs(kc + wbase, BSTR + WIN * DH * 2), rv = mkrs(vc + wbase, BSTR + WIN * DH * 2);
    const int voff = ((mem * 8 + w) * 8 + kk) * (DH * 2) + 16 * sub;
    u32x4 tl[NBUF][3];
    u64 tst0;
    asm volatile("s_memrealtime %0" : "=s"(tst0));
    unsigned tstart;
    unsigned late = 0, dstep = 0;
    unsigned xd = 0, sl = 0;
#define PACE(t) do { if ((t) >= PF) { \
      if ((t) >= NBUF && (t) < NBUF + (1 << RSH)) tstart += (((int)dstep * ((t) - NBUF + 1)) >> RSH) - (((int)dstep * ((t) - NBUF)) >> RSH); \
      { unsigned xv_; asm volatile("s_add_u32 %0, %0, %1" : "+s"(tstart) : "s"(sl) : "scc");     \
      asm volatile("1%=:\n\tv_mov_b32 %1, %6\n\tds_read_b32 %1, %1\n\ts_memrealtime s[90:91]\n\ts_waitcnt lgkmcnt(0)\n\tv_readfirstlane_b32 %2, %1\n\ts_sub_u32 %0, s90, %4\n\t" \
                   "s_and_b32 %3, %2, 255\n\ts_lshr_b32 %2, %2, 8\n\ts_addk_i32 %0, %5\n\ts_sub_u32 %0, %0, %2\n\t" \
                   "s_cmp_lt_i32 %0, 0\n\ts_cbranch_scc0 2%=f\n\ts_sleep 1\n\ts_branch 1%=b\n2%=:" \
                   : "=&s"(late), "=&v"(xv_), "=&s"(xd), "=&s"(sl) : "s"(tstart), "n"(-sched(t)), "s"((unsigned)(uintptr_t)&ctl[14]) : "scc", "s90", "s91", "memory"); \
      } \
      if (ckof(t) >= 0) { unsigned oc_;     \
        asm volatile("s_getreg_b32 %0, hwreg(HW_REG_IB_STS, 0, 4)" : "=s"(oc_)); \
        if (ckof(t) >= 0) asm volatile("s_mov_b64 exec, 1\n\tds_add_u32 %0, %1\n\ts_mov_b64 exec, -1" :: "v"((unsigned)(uintptr_t)&ctl[16 + (ckof(t) & 3)]), "v"(PCAP - min(10 * oc_ + late, (unsigned)(ckof(t) == 0 ? CP0 : ckof(t) == 1 ? CP1 : CP2))) : "memory"); \
      } \
      if (PR1) asm volatile("s_setprio 0\n\ts_cmpk_lt_u32 %0, %1\n\ts_cbranch_scc1 1%=f\n\ts_setprio 1\n\ts_cmpk_lt_u32 %0, %2\n\ts_cbranch_scc1 1%=f\n\t" \
                   "s_setprio 2\n\ts_cmpk_lt_u32 %0, %3\n\ts_cbranch_scc1 1%=f\n\ts_setprio 3\n1%=:" \
                   :: "s"(late), "n"(PR1), "n"(PR2), "n"(PR3) : "scc", "memory"); } } while (0)
#define WINDOW(t) do { if (LATE && (t) >= PF && (t) < TSW) \
      asm volatile("s_cmpk_lt_u32 %0, %1\n\ts_cbranch_scc0 1%=f\n\ts_waitcnt vmcnt(%2)\n1%=:\n\ts_waitcnt vmcnt(%3)" \
                   :: "s"(late), "n"(LATE), "n"(WINL - 1), "n"(WINB - 1) : "scc", "memory"); \
      else VMW(((t) < TSW ? WINL : WINV) - 1); } while (0)
#define ISSUE_T(t) do { PACE(t); _Pragma("unroll") for (int i_ = 0; i_ < 3; ++i_) { \
      WINDOW(t); \
      tl[(t) % NBUF][i_] = ldb_nt((t) < NT ? rk : rv, voff + 128 * i_, ((((t) >> 3) & 1) ^ 1) * BSTR + ((t) & 7) * TSTR); \
      SB(); } } while (0)
    SB();
    ISSUE_T(0);
    asm volatile("s_waitcnt lgkmcnt(0)" : "+s"(tst0));
    clk[w] = (unsigned)tst0;
    int tfst = 0;
    if (w == 0) { VMW(2); tfst = (int)((unsigned)__builtin_amdgcn_s_memrealtime() - (unsigned)tst0); }
    BAR();
    tstart = __builtin_amdgcn_readfirstlane(clk[1]) + TOFS + PHS;
    if (w == 0) ctl[14] = (unsigned)(min(max((tfst - L0) * LGN >> 3, 0), RMX) << 8);
#pragma unroll
    for (int t = 1; t < NBUF; ++t) ISSUE_T(t);
    SB();
    LDSWAIT(ctl[13], 1, 2);
    dstep = __builtin_amdgcn_readfirstlane(ctl[12]);
    u32x4 qp[3];
    float sc[NT];
#pragma unroll
    for (int t = 0; t < NT; ++t) {
      SB();
      if ((t & 7) == 0) {
#pragma unroll
        for (int i = 0; i < 3; ++i) qp[i] = *reinterpret_cast<const u32x4*>(&qs[(t >> 3) ^ 1][32 * i + 4 * sub]);
      }
      float d0 = 0.f, d1 = 0.f;
#pragma unroll
      for (int i = 0; i < 3; ++i) {
        d0 = dot2(tl[t % NBUF][i][0], qp[i][0], d0); d1 = dot2(tl[t % NBUF][i][1], qp[i][1], d1);
        d0 = dot2(tl[t % NBUF][i][2], qp[i][2], d0); d1 = dot2(tl[t % NBUF][i][3], qp[i][3], d1);
      }
      asm volatile("" : "+v"(d0), "+v"(d1));
      SB();
      ISSUE_T(t + NBUF);
      SB();
      float d = d0 + d1;
      d += dpp<XOR1>(d); d += dpp<XOR2>(d); d += dpp<XOR7>(d);
      sc[t ^ 8] = rbf(rbf(d) * SCALE);
    }
    float m[2];
#pragma unroll
    for (int bi = 0; bi < 2; ++bi) {
      float mm = sc[8 * bi], l = 0.f;
#pragma unroll
      for (int t = 1; t < 8; ++t) mm = fmaxf(mm, sc[8 * bi + t]);
      mm = fmaxf(mm, dpp<XOR8>(mm)); mm = x16max(mm); mm = x32max(mm);
      m[bi] = mm * LOG2E;
#pragma unroll
      for (int t = 8 * bi; t < 8 * bi + 8; ++t) { sc[t] = __builtin_amdgcn_exp2f(fmaf(sc[t], LOG2E, -m[bi])); l += sc[t]; }
      mlw[0][8 * bi + w] = m[bi]; mlw[1][8 * bi + w] = sum_kk(l);
    }
    LDSINC(ctl[4]);
    u32x4 msk;
#pragma unroll
    for (int r = 0; r < 4; ++r) msk[r] = (sub >> 1) == r ? ((sub & 1) ? 0xffff0000u : 0x0000ffffu) : 0u;
    f32x4 acc[3];
    unsigned mwr = (unsigned)(uintptr_t)&mrg[0][w][32 * ((lane >> 4) & 1) + 4 * sub + 2 * (lane >> 5)];
    unsigned mrd = (unsigned)(uintptr_t)&mrg[1][0][4 * (lane < 48 ? lane : 0)];
    int opub = lane < 48 ? WS_O + (((wi & 31) * NB + (wi >> 5)) * 4 + mem) * (DH * 4) + 16 * ((lane & 48) + 2 * sub + ((lane >> 3) & 1)) : WS_END;
#define DEPOSIT(tick, ...) do { float a_[12]; _Pragma("unroll") for (int i_ = 0; i_ < 12; ++i_) a_[i_] = acc[i_ >> 2][i_ & 3]; \
      asm volatile("s_nop 5\n\tv_permlane32_swap_b32 %0, %2\n\tv_permlane32_swap_b32 %1, %3\n\tv_permlane32_swap_b32 %4, %6\n\tv_permlane32_swap_b32 %5, %7\n\t" \
                   "v_permlane32_swap_b32 %8, %10\n\tv_permlane32_swap_b32 %9, %11\n\t" \
                   "v_add_f32_dpp %0, %2, %0 row_ror:8 row_mask:0xf bank_mask:0xf\n\tv_add_f32_dpp %1, %3, %1 row_ror:8 row_mask:0xf bank_mask:0xf\n\t" \
                   "v_add_f32_dpp %4, %6, %4 row_ror:8 row_mask:0xf bank_mask:0xf\n\tv_add_f32_dpp %5, %7, %5 row_ror:8 row_mask:0xf bank_mask:0xf\n\t" \
                   "v_add_f32_dpp %8, %10, %8 row_ror:8 row_mask:0xf bank_mask:0xf\n\tv_add_f32_dpp %9, %11, %9 row_ror:8 row_mask:0xf bank_mask:0xf\n\t" \
                   "s_mov_b64 exec, %[vm]\n\tds_write2_b32 %[a], %0, %1 offset1:1\n\tds_write2_b32 %[a], %4, %5 offset0:64 offset1:65\n\tds_write2_b32 %[a], %8, %9 offset0:128 offset1:129\n\t" \
                   "s_mov_b64 exec, 1\n\t" tick "\n\ts_mov_b64 exec, -1" \
                   : "+v"(a_[0]), "+v"(a_[1]), "+v"(a_[2]), "+v"(a_[3]), "+v"(a_[4]), "+v"(a_[5]), "+v"(a_[6]), "+v"(a_[7]), "+v"(a_[8]), "+v"(a_[9]), "+v"(a_[10]), "+v"(a_[11]), [o] "=&v"(old) \
                   : [vm] "s"(0x00ff00ff00ff00ffull), [a] "v"(mwr), [one] "v"(1u), __VA_ARGS__ : "memory"); } while (0)
    unsigned old;
#pragma unroll
    for (int i = 0; i < 3; ++i) acc[i] = f32x4{0.f, 0.f, 0.f, 0.f};
#pragma unroll
    for (int t = 0; t < NT; ++t) {
      SB();
      if (t == 7) {
        LDSWAIT(ctl[4], 8, 1);
        tag = mktag(__builtin_amdgcn_readfirstlane(ctl[0]));
#pragma unroll
        for (int bi = 0; bi < 2; ++bi) {
          const f32x4 ma = *reinterpret_cast<const f32x4*>(&mlw[0][8 * bi]), mb = *reinterpret_cast<const f32x4*>(&mlw[0][8 * bi + 4]);
          const float M = rnd22(fmaxf(fmaxf(fmaxf(ma[0], ma[1]), fmaxf(ma[2], ma[3])), fmaxf(fmaxf(mb[0], mb[1]), fmaxf(mb[2], mb[3]))));
          const float wg = __builtin_amdgcn_exp2f(m[bi] - M);
          if (bi == 1) {
#pragma unroll
            for (int i = 0; i < 3; ++i) acc[i] *= wg;
          }
#pragma unroll
          for (int u = bi ? 15 : 0; u < 8 * bi + 8; ++u) sc[u] *= wg;
        }
        asm volatile("" : "+v"(mwr), "+v"(mrd), "+v"(opub), "+s"(tag.t[0]), "+s"(tag.t[1]), "+s"(tag.t[2]), "+s"(tag.t[3]));
        SB();
      }
      const unsigned pd = cvtpk(sc[t ^ 8], sc[t ^ 8]);
      const u32x4 af = {pd & msk[0], pd & msk[1], pd & msk[2], pd & msk[3]};
#pragma unroll
      for (int i = 0; i < 3; ++i) acc[i] = mfma(af, tl[(t + NT) % NBUF][i], acc[i]);
      SB();
      if (t == 7) {
        DEPOSIT("ds_add_u32 %[c], %[one]", [c] "v"((unsigned)(uintptr_t)&ctl[10]));
        mwr += 8 * DH * 4;
#pragma unroll
        for (int i = 0; i < 3; ++i) acc[i] = f32x4{0.f, 0.f, 0.f, 0.f};
      }
      if (viss(t)) ISSUE_T(viss(t));
      if (viss(t) == WTRIG) { unsigned tv_; asm volatile("s_add_u32 %1, %1, %2\n\tv_mov_b32 %0, %1" : "=v"(tv_), "+s"(tstart) : "s"(xd) : "scc"); clk[w] = tv_; LDSINC(ctl[8]); }
      SB();
    }
    {
      f32x4 pv[8];
      DEPOSIT("ds_add_rtn_u32 %[o], %[c], %[one]", [c] "v"((unsigned)(uintptr_t)&ctl[5]));
      typedef __attribute__((address_space(3))) const f32x4* lp4;
      asm volatile("s_waitcnt lgkmcnt(0)" : "+v"(old) :: "memory");
      const unsigned tk = __builtin_amdgcn_readfirstlane(old);
      if (tk == 7u || tk == ET) {
        if (__builtin_expect(tk == ET, 0)) { LDSWAIT(ctl[10], 8, 1); mrd -= 8 * DH * 4; opub += 2 * 4 * DH * 4; }
#pragma unroll
        for (int i = 0; i < 8; ++i) pv[i] = *reinterpret_cast<lp4>(mrd + i * (DH * 4));
        SB();
#pragma unroll
        for (int st = 1; st < 8; st *= 2)
#pragma unroll
          for (int i = 0; i < 8; i += 2 * st) pv[i] += pv[i + st];
        st_ag(rw, opub, g_pack(pv[0][0], pv[0][1], pv[0][2], pv[0][3], tag));
      }
    }
    if (proj || ffn) {
      ROLES();
      const int j = lane & 15, g = lane >> 4;
      typedef __attribute__((address_space(3))) const u32x4* lpu;
      const int wd = (cb * 8 + w) * 64 + lane;
      unsigned ydst = WS_Y + (wd / YP) * YL + (wd % YP) * 8;
      unsigned oo = (g * DM + 128 * cb + 8 * j + w) * 2;
      unsigned ard = (unsigned)(uintptr_t)&oA[j >> 2][4 * g + 16 * (j & 3)], brd = (unsigned)(uintptr_t)&wf[0][w][lane];
      unsigned br2 = (unsigned)(uintptr_t)&wf[0][w][0] + 8 * lane;
      auto rdb = [&](int ks) __attribute__((always_inline)) {
        if (ks < 3) return *reinterpret_cast<lpu>(brd + 8192 * ks);
        typedef __attribute__((address_space(3))) const u32x2* lp2;
        const u32x2 a = *reinterpret_cast<lp2>(br2 + 8192 * ks), b = *reinterpret_cast<lp2>(br2 + 8192 * ks + 512);
        return u32x4{a[0], a[1], b[0], b[1]};
      };
      u32x4 bw[6], af[2];
      f32x4 y[3];
      auto finish = [&](const float yv) __attribute__((always_inline)) {
        u64 add = __builtin_bit_cast(u64, __builtin_fma((double)yv, 274877906944.0, 6755399441055744.0)) + ((1ull << 57) - 0x4338000000000000ull);
        const bool side = (__float_as_uint(yv) << 1) - (YLO << 24) > ((YHI - YLO) << 24);
        asm volatile("" : "+v"(add));
        if (__builtin_expect(__builtin_amdgcn_ballot_w64(side) != 0, 0)) {
          if (side) {
            __builtin_amdgcn_raw_buffer_store_b64(u32x2{__float_as_uint(yv), ctl[0]}, rw, WS_S + ((proj ? grp : NH + grp) * (NB * DM) + wd) * 8, 0, AUX_SC1);
            __hip_atomic_fetch_or(reinterpret_cast<u64*>(ws + ydst), 1ull << 63, __ATOMIC_RELAXED, __HIP_MEMORY_SCOPE_AGENT);
            add = 1ull << 57;
          }
          VMW(0);
          asm volatile("" : "+v"(ydst));
        }
        u64 tot = __hip_atomic_fetch_add(reinterpret_cast<u64*>(ws + ydst), add, __ATOMIC_RELAXED, __HIP_MEMORY_SCOPE_AGENT);
        SB();
        add += (1ull << 31) - ((u64)NY << 57);
        asm volatile("" : "+v"(add));
        SB();
        tot += add;
        asm volatile("" : "+v"(tot));
        const int hi = (int)(tot >> 31) >> 1;
        if ((unsigned)(hi + (1 << 24)) < (1u << 25)) {
          float z = fmaf((float)hi, 1.f / 64, (float)(int)((unsigned)tot ^ 0x80000000u) * (1.f / 274877906944.f));
          if (__builtin_expect(hi != (int)(tot >> 32), 0)) {
#pragma unroll 1
            for (int a = 0; a < NY; ++a) {
              const u32x2 v = __builtin_amdgcn_raw_buffer_load_b64(rw, WS_S + (a * (NB * DM) + wd) * 8, 0, AUX_SC1);
              if (v[1] == ctl[0]) z += __uint_as_float(v[0]);
            }
          }
          *reinterpret_cast<unsigned short*>(reinterpret_cast<unsigned char*>(out) + oo) = (unsigned short)cvtpk(z, 0.f);
        }
      };
      if (proj && w < 3) {
        const int bb = lane >> 4, dq = 16 * w + (lane & 15);
        const int f0 = WS_O + (grp * NB + bb) * 4 * (DH * 4) + 16 * dq;
        int f[4] = {f0, f0 + DH * 4, f0 + 2 * DH * 4, f0 + 3 * DH * 4};
        unsigned awr = (unsigned)(uintptr_t)&oA[bb][16 * (w + 3 * ((lane >> 3) & 1)) + 2 * (lane & 7)];
        f32x4 r[4]; u32x4 p[12];
        LDSWAIT(ctl[9], 1, 1);
        f32x4 sq = *reinterpret_cast<const f32x4*>(&mlw[2][4 * bb]);
        asm volatile("" : "+v"(sq), "+v"(awr), "+v"(ydst), "+v"(ard), "+v"(brd), "+v"(br2), "+v"(oo), "+v"(f[0]), "+v"(f[1]), "+v"(f[2]), "+v"(f[3]));
        tstart += sched(31) - PHS + max(PT1 + (((AO ? 32 + grp : 2 * grp) * SPAN) >> 6), WB + PD + ((grp * WPB) >> 5));
        while ((int)((unsigned)__builtin_amdgcn_s_memrealtime() - tstart) < 0) __builtin_amdgcn_s_sleep(1);
        poll4<PSL1, FW1>(r, p, f, rw, tag);
        const f32x4 o = (sq[0] * r[0] + sq[1] * r[1]) + (sq[2] * r[2] + sq[3] * r[3]);
        *reinterpret_cast<__attribute__((address_space(3))) u32x2*>(awr) = u32x2{cvtpk(o[0], o[1]), cvtpk(o[2], o[3])};
        BAR();
        af[0] = *reinterpret_cast<lpu>(ard); af[1] = *reinterpret_cast<lpu>(ard + 192);
#pragma unroll
        for (int ks = 0; ks < 6; ++ks) bw[ks] = rdb(ks);
        SB();
#pragma unroll
        for (int i = 0; i < 3; ++i) y[i] = mfma(af[0], bw[i], f32x4{0.f, 0.f, 0.f, 0.f});
#pragma unroll
        for (int i = 0; i < 3; ++i) y[i] = mfma(af[1], bw[3 + i], y[i]);
        finish((y[0][0] + y[1][1]) + y[2][2]);
        poll4_keep(p);
      } else {
        LDSWAIT(ctl[15], 3, 1);
#pragma unroll
        for (int ks = 0; ks < 3; ++ks) bw[ks] = *reinterpret_cast<lpu>(brd + 8192 * ks);
#pragma unroll
        for (int ks = 0; ks < 3; ++ks) asm volatile("" : "+v"(bw[ks]));
        asm volatile("" : "+v"(ydst), "+v"(ard), "+v"(oo), "+v"(br2));
        LDSWAIT(ctl[6], 4, 0);
        if (proj) {
#pragma unroll
          for (int ks = 3; ks < 6; ++ks) bw[ks] = rdb(ks);
          SB();
          asm volatile("s_barrier" ::: "memory");
          SB();
          af[0] = *reinterpret_cast<lpu>(ard); af[1] = *reinterpret_cast<lpu>(ard + 192);
          SB();
#pragma unroll
          for (int i = 0; i < 3; ++i) y[i] = mfma(af[0], bw[i], f32x4{0.f, 0.f, 0.f, 0.f});
#pragma unroll
          for (int i = 0; i < 3; ++i) y[i] = mfma(af[1], bw[3 + i], y[i]);
        } else {
          const float xs = bflo(reinterpret_cast<const unsigned short*>(&red[0][0][0])[8 * lane + w]);
          af[0] = *reinterpret_cast<lpu>(ard);
#pragma unroll
          for (int i = 0; i < 3; ++i) y[i] = mfma(af[0], bw[i], f32x4{i == 0 ? xs : 0.f, 0.f, 0.f, 0.f});
        }
        finish((y[0][0] + y[1][1]) + y[2][2]);
      }
    }
  } else {
    LAND(0);
    const int w = wave;
    unsigned tstart;
    const unsigned town = (unsigned)__builtin_amdgcn_s_memrealtime() + SOFS;
    if (PRS) __builtin_amdgcn_s_setprio(PRS);
    const rsrc_t rc = mkrs(reinterpret_cast<const void*>(pc0 & ~255ull), CODEB);
    const int cofs = 1024 * (blockIdx.x >> 3);
    u32x4 cv = {0u, 0u, 0u, 0u};
    if (CPE && w == 3) cv = ldb(rc, 16 * lane, cofs);
    {
      const int j = lane & 15, g = lane >> 4;
      const bool hq = (h ^ (h >> HQX)) & 1;
      const bool isq = hq || sp >= 4;
      const int fi = sp, cb0 = 3 * (h >> 1);
      const int qu = hq ? sp + 4 : sp - 4, qh = (h & 30) + (qu >= 6), qcol = qh * DH + 64 * ((qu >= 6 ? qu - 6 : qu) >> 1);
      const int ns = isq ? 5 : 8 - (fi & 1), n1 = isq ? 5 : (0x7528 >> (4 * fi)) & 15, s0 = isq ? 5 * (qu & 1) : (15 * fi + 1) >> 1;
      const int ld2 = isq ? NH * DH * 2 : FF * 2;
      const rsrc_t rx = mkrs(x, NB * DM * 2);
      const int vo = (32 * (j >> 3) + 8 * g) * ld2 + 16 * (j & 7), xo = ((j & 3) * DM + 32 * ((j >> 2) & 1) + 8 * g) * 2;
      u32x4 wv[2][8], xa[2], xr[2];
      LAND(1);
      if (ZE && (blockIdx.x >> 3) == 16 && lane < 40) {
        const int wd = 320 * (blockIdx.x & 7) + 80 * w + 2 * lane;
        st_ag(rw, WS_Y + (wd / YP) * YL + (wd % YP) * 8, u32x4{0u, 0u, 0u, 0u});
      }
      const u32x2 ep = __builtin_amdgcn_raw_buffer_load_b64(rw, WS_EPOCH, 0, AUX_SC1);
#pragma unroll
      for (int i = 0; i < 2; ++i) xr[i] = ldb(rx, (w * DM + 16 * (lane < 40 ? lane : 0) + 8 * i) * 2, 0);
#pragma unroll
      for (int st = 0; st < 2; ++st) {
        const int gs = s0 + w + 4 * st, cw = cb0 + (gs >= 10) + (gs >= 20), rs = gs - 10 * ((gs >= 10) + (gs >= 20));
        xa[st] = ldb(rx, xo, 128 * rs);
        if (st == 1) LAND(2);
        if (st == 0 || w + 4 < ns) {
          const bf16* um = isq ? wq + qcol : (cw < 24 ? w1 + 64 * cw : w3 + 64 * (cw - 24));
          const rsrc_t ru = mkrs(um, (unsigned)(DM - 1) * ld2 + 128);
#pragma unroll
          for (int e = 0; e < 8; ++e) { wv[st][e] = ldb_nt(ru, vo, (64 * rs + e) * ld2); SB(); }
        }
      }
      LAND(3);
      if (!ZE && (blockIdx.x >> 3) == 16 && lane < 40) {
        const int wd = 320 * (blockIdx.x & 7) + 80 * w + 2 * lane;
        st_ag(rw, WS_Y + (wd / YP) * YL + (wd % YP) * 8, u32x4{0u, 0u, 0u, 0u});
      }
      if (tid < 18) ctl[2 + tid] = 0u;
      SB();
      VMW(12);
      BAR();
      SB();
      const unsigned tst0 = clk[1];
      const u64 epoch = ((u64)__builtin_amdgcn_readfirstlane(ep[0]) | ((u64)__builtin_amdgcn_readfirstlane(ep[1]) << 32)) + 1ull;
      tag = mktag((unsigned)epoch);
      u32x4 stg = s_tag((unsigned)epoch, lane);
      const int da = isq ? WS_Q + ((qu & 1) * (NH * DH) + qcol) * 8 : WS_GU + ((fi > 0 ? 2 * FF : 0) + 64 * (cb0 + (fi > 0 ? fi - 1 : 0))) * 8;
      int dst = (lane < 32 ? da : n1 < ns ? WS_GU + 64 * (cb0 + fi) * 8 : WS_END) + 16 * (lane & 31);
      unsigned rdr = (unsigned)(uintptr_t)&red[lane >> 5][2 * (lane & 3)][(lane & 31) >> 2];
      asm volatile("" : "+v"(stg), "+v"(dst), "+v"(rdr), "+s"(tag.t[0]), "+s"(tag.t[1]), "+s"(tag.t[2]), "+s"(tag.t[3]));
      {
        float v[16]; float sm = 0.f;
#pragma unroll
        for (int i = 0; i < 2; ++i)
#pragma unroll
          for (int k = 0; k < 4; ++k) { v[8 * i + 2 * k] = bflo(xr[i][k]); v[8 * i + 2 * k + 1] = bfhi(xr[i][k]); sm += v[8 * i + 2 * k] + v[8 * i + 2 * k + 1]; }
        const float mean = wave_sum(lane < 40 ? sm : 0.f) * (1.f / DM);
        float sq = 0.f;
#pragma unroll
        for (int k = 0; k < 16; ++k) { v[k] -= mean; sq += v[k] * v[k]; }
        const float var = wave_sum(lane < 40 ? sq : 0.f) * (1.f / DM);
        stat[w][0] = rbf(mean); stat[w][1] = 1.f / rbf(sqrtf(rbf(rbf(var) + 1e-5f)));
        LDSINC(ctl[7]);
        LDSWAIT(ctl[7], 4, 0);
      }
      SB();
      const float mean = stat[j & 3][0], rinv = stat[j & 3][1];
#pragma unroll
      for (int st = 0; st < 2; ++st) {
        if (st == 0 || w + 4 < ns) {
          u32x4 af;
#pragma unroll
          for (int p = 0; p < 4; ++p) {
            const unsigned c = cvtpk(bflo(xa[st][p]) - mean, bfhi(xa[st][p]) - mean);
            af[p] = cvtpk(bflo(c) * rinv, bfhi(c) * rinv);
          }
          f32x4 d[8];
#pragma unroll
          for (int ec = 0; ec < 8; ++ec) d[ec] = mfma(af, bfrag(&wv[st][0], ec), f32x4{0.f, 0.f, 0.f, 0.f});
          if (g == (j >> 3)) {
#pragma unroll
            for (int ec = 0; ec < 8; ++ec) red[2 * (w + 4 * st) + g][ec][j & 7] = d[ec];
          }
        }
      }
      tstart = __builtin_amdgcn_readfirstlane(tst0);
      asm volatile("" :: "v"(cv));
      if (tid == 0) *reinterpret_cast<u64*>(&ctl[0]) = epoch;
      asm volatile("s_waitcnt vmcnt(0) lgkmcnt(0)" ::: "memory");
      unsigned old = 0;
      if (lane == 0) old = __hip_atomic_fetch_add(&ctl[2], 1u, __ATOMIC_RELAXED, __HIP_MEMORY_SCOPE_WORKGROUP);
      const bool last = __builtin_amdgcn_readfirstlane(old) == 3u;
      FENCE();
      if (__builtin_amdgcn_readfirstlane(old) == 0u) {
        typedef const unsigned char* cp;
        ROLES();
        const size_t wb = ((size_t)wi * SK + (SK - WIN)) * DH * 2 + (j & 8) * 49152 + (j & 1) * 393088;
        const cp tp = g == 2 && TOL >= 0 ? ws + WS_O + (((wi & 31) * NB + (wi >> 5) + (j < 6 ? 0 : 2)) * 4 + mem) * (DH * 4) + TOL * (j < 6 ? j : j < 12 ? j - 6 : 0)
                    : g == 3 ? (j < 15 || !TCK ? ws + WS_Y + ((j & TYM) * (WS_S - WS_Y) / 15 & ~255) + 192 : ws + WS_CK + (TCK == 2 ? (blockIdx.x >> 4 & 3) * 8192 + (blockIdx.x & 15) * 128 : blockIdx.x * 128))
                    : mem == 0 ? (cp)kc + wb : mem == 1 ? (cp)vc + wb : mem == 2 ? (cp)vc + wb + BSTR
                    : j < 8 && proj ? (cp)wo + min(DH * (((ru8 & ~7) * 205) >> 10) + 36 * ((2 * j + pr) & TWM), NH * DH - 1) * (DM * 2)
                    : j < 8 && ffn ? (cp)w2 + min(96 * (((ru8 & ~7) * 205) >> 10) + 18 * ((2 * j + pr) & TWM), FF - 1) * (DM * 2)
                    : ws + WS_O + min(j & TOM, 5) * 65536 + 65472;
        unsigned tv;
        asm volatile("global_load_dword %0, %1, off sc1\n\ts_waitcnt vmcnt(0)" : "=&v"(tv) : "v"(tp) : "memory");
      }
      if (last) {
        {
          typedef __attribute__((address_space(3))) const f32x4* lp4;
          u32x4 gs;
          f32x4 v[8][2], va[2], vb[2] = {};
#pragma unroll
          for (int p = 0; p < 5; ++p) { v[p][0] = *reinterpret_cast<lp4>(rdr + 2304 * p); v[p][1] = *reinterpret_cast<lp4>(rdr + 2304 * p + 144); }
          if (isq) {
            SB();
#pragma unroll
            for (int k = 0; k < 2; ++k) va[k] = ((v[0][k] + v[1][k]) + (v[2][k] + v[3][k])) + v[4][k];
          } else {
#pragma unroll
            for (int p = 5; p < 8; ++p) { const unsigned a = rdr + 2304 * (p == 7 ? ns - 1 : p); v[p][0] = *reinterpret_cast<lp4>(a); v[p][1] = *reinterpret_cast<lp4>(a + 144); }
            SB();
#pragma unroll
            for (int k = 0; k < 2; ++k) {
              va[k] = v[0][k] + v[1][k];
#pragma unroll
              for (int p = 2; p < 8; ++p) { va[k] += (p < n1 ? 1.f : 0.f) * v[p][k]; vb[k] += (p >= n1 && p < ns ? 1.f : 0.f) * v[p][k]; }
            }
          }
#pragma unroll
          for (int k = 0; k < 2; ++k) {
            const u32x2 pk = s_pack(f32x4{tr32(va[k][0], vb[k][0]), tr32(va[k][1], vb[k][1]), tr32(va[k][2], vb[k][2]), tr32(va[k][3], vb[k][3])}, stg[2 * k], stg[2 * k + 1]);
            gs[2 * k] = pk[0]; gs[2 * k + 1] = pk[1];
          }
          st_ag(rw, dst, gs);
        }
        if ((blockIdx.x >> 3) == 16 && lane < TREP) st_ag(rw, WS_TM + 128 * lane + 16 * (blockIdx.x & 7), g_int(tstart, ((unsigned)__builtin_amdgcn_s_memrealtime() - tstart) & 4095u, tag));
        while ((unsigned)__builtin_amdgcn_s_memrealtime() - town < (unsigned)QT) __builtin_amdgcn_s_sleep(4);
        const int qo = WS_Q + (wi & 31) * DH * 8 + 16 * (lane < 48 ? lane : lane & 3);
        const int qo0 = lane >= 48 ? WS_TM + 128 * (h & (TREP - 1)) + 16 * (lane & 7) : qo;
        const u32x4 tg0 = lane < 48 ? stg : u32x4{tag.t[0], tag.t[1], tag.t[2], tag.t[3]};
        u32x4 g0, g1, g2, g3;
#pragma unroll 1
        for (int it = 0; it < SPIN_MAX; ++it) {
          FENCE();
          g0 = ld_ag(rw, qo0); g1 = ld_ag(rw, qo + NH * DH * 8);
          g2 = ld_ag(rw, qo + 768); g3 = ld_ag(rw, qo + NH * DH * 8 + 768);
          if (__all(((s_dif(g0, tg0) & (lane < 48 ? 15u : 1023u)) | ((s_dif(g1, stg) | s_dif(g2, stg) | s_dif(g3, stg)) & 15u)) == 0u)) break;
          __builtin_amdgcn_s_sleep(QBACK);
        }
        {
          const float d = fminf(fmaxf((float)((int)((g0[0] & ~1023u) - (tstart << 10)) >> 10), -300.f), 300.f);
          float sm = d + dpp<XOR1>(d); sm += dpp<XOR2>(sm); sm += dpp<XOR7>(sm);
          float mx = fminf(d, dpp<XOR1>(d)); mx = fminf(mx, dpp<XOR2>(mx)); mx = fminf(mx, dpp<XOR7>(mx));
          float lx = fmaxf(d, dpp<XOR1>(d)); lx = fmaxf(lx, dpp<XOR2>(lx)); lx = fmaxf(lx, dpp<XOR7>(lx));
          const float db = rintf(fmaxf(sm * (0.125f - CAL / 64.f) + mx * (CAL / 8.f), lx - CUP));
          if (lane == 48) { ctl[12] = (unsigned)(int)db; ctl[11] = (unsigned)(int)(lx - db); }
          float rd = (float)(g0[2] >> 10); rd += dpp<XOR1>(rd); rd += dpp<XOR2>(rd); rd += dpp<XOR7>(rd);
          if (lane == 48) ctl[14] = ((unsigned)fminf(fmaxf((rd * 0.125f - R0) * (RG / 8.f), 0.f), RMX) << 8);
        }
#pragma unroll
        for (int r = 0; r < 2; ++r) {
          const u32x4 ga = r ? g2 : g0, gb = r ? g3 : g1;
          float q[2][2];
#pragma unroll
          for (int k = 0; k < 2; ++k) {
            const float sa = s_scale(ga[2 * k], ga[2 * k + 1]), sb = s_scale(gb[2 * k], gb[2 * k + 1]);
            const unsigned a = wi < NH ? ga[2 * k] : ga[2 * k + 1], b = wi < NH ? gb[2 * k] : gb[2 * k + 1];
            q[0][k] = fmaf(s_hi(a), sa, s_hi(b) * sb); q[1][k] = fmaf(s_lo(a), sa, s_lo(b) * sb);
          }
          if (lane < 48) { qs[0][48 * r + lane] = cvtpk(q[0][0], q[0][1]); qs[1][48 * r + lane] = cvtpk(q[1][0], q[1][1]); }
        }
      }
      if (last) LDSINC(ctl[13]);
      LDSWAIT(ctl[13], 1, 8);
      tstart += __builtin_amdgcn_readfirstlane(ctl[12]);
    }
    if (NCK && w == 3) {
      unsigned xt = __builtin_amdgcn_readfirstlane(ctl[14]) >> 8, sl = 0;
      float pp = 0.f;
      const int ds = __builtin_amdgcn_readfirstlane(ctl[12]), dl = __builtin_amdgcn_readfirstlane(ctl[11]);
#pragma unroll 1
      for (int k = 0; k < NCK; ++k) {
        const int tl = ckt(k) + 1;
        const unsigned tp = tstart + TOFS + SPAN + CKO + sched(tl) + xt + 2 * sl;
        const int rem = min(max(NBUF - 1 + (1 << RSH) - tl, 0), 1 << RSH);
        while ((int)((unsigned)__builtin_amdgcn_s_memrealtime() - tp) < -((ds * rem) >> RSH)) __builtin_amdgcn_s_sleep(8);
        FENCE();
        const unsigned cr = ctl[16 + k];
        if ((h >> 3) == (sp & 3) && ((sp ^ (h >> 1)) & RPM) == 0 && lane < NREP) st_ag(rw, WS_CK + k * 8192 + lane * 1024 + (RPM == 0 ? sp * 8 + (h & 7) : RPM == 4 ? (sp & 3) * 8 + (h & 7) : (sp & 1) * 8 + (h & 7)) * 16, g_int(cr, 0u, tag));
        while ((int)((unsigned)__builtin_amdgcn_s_memrealtime() - tp) < CRD + ((dl * rem) >> RSH)) __builtin_amdgcn_s_sleep(4);
        u32x4 g0;
#pragma unroll 1
        for (int it = 0; it < 4; ++it) {
          FENCE();
          g0 = ld_ag(rw, WS_CK + k * 8192 + (h & (NREP - 1)) * 1024 + (lane & (RPM == 0 ? 63 : RPM == 4 ? 31 : 15)) * 16);
          if (__all(g_ok(g0, tag))) break;
        }
        const float p = PCAP - wave_sum(g_ok(g0, tag) ? (float)min(g0[0] >> 10, 4096u) : 0.f) * (1.f / 1024);
        const float df = k == 0 ? 0.1f * CG0 * (p - CZ0) : k < NCK - 1 ? 0.1f * CG1 * (p - CZ1)
                                 : fminf(fmaxf(0.1f * MA * (p - MZ), 0.1f * MB * (p - MY)), MC + 0.1f * MD * p);
        const unsigned d = (unsigned)fmaxf(fminf(df, 250.f), 0.f);
        xt = __builtin_amdgcn_readfirstlane(xt + d);
        if (k == 1) sl = __builtin_amdgcn_readfirstlane(p > SLZ && p > pp - SLD ? SLM : 0);
        if (k == 2 && p < SLY) sl = 0;
        pp = p;
        if (lane == 0) ctl[14] = (xt << 8) | sl;
      }
    }
    if (CPL && w == 2) {
      LDSWAIT(ctl[4], 1, 16);
      cv = ldb(rc, 16 * lane, cofs);
      asm volatile("" :: "v"(cv));
    }
    if (w == 1) {
      LDSWAIT(ctl[4], 8, 16);
      const float mi = mlw[0][lane & 15], li = mlw[1][lane & 15];
      float M = fmaxf(mi, dpp<XOR1>(mi)); M = fmaxf(M, dpp<XOR2>(M)); M = rnd22(fmaxf(M, dpp<XOR7>(M)));
      float L = li * __builtin_amdgcn_exp2f(mi - M);
      L += dpp<XOR1>(L); L += dpp<XOR2>(L); L += dpp<XOR7>(L);
      const u32x4 gp = g_pack(M, L, dpp<XOR8>(M), dpp<XOR8>(L), tag);
      if (lane == 0) st_ag(rw, WS_ML + (wi & 31) * MLH + (2 * mem + (wi >> 5)) * MLS, gp);
      if (blockIdx.x == 255) {
        const unsigned xw = __builtin_amdgcn_readfirstlane(ctl[14]), tml = tstart + (xw >> 8) + 10 * (xw & 255u);
        while ((unsigned)__builtin_amdgcn_s_memrealtime() - tml < (unsigned)sched(24) + TOFS + MLD + 30) __builtin_amdgcn_s_sleep(8);
        bool ok = false;
#pragma unroll 1
        for (int it = 0; it < SPIN_MAX && !ok; ++it) {
          FENCE();
          ok = true;
#pragma unroll
          for (int i = 0; i < 4; ++i) ok &= g_ok(ld_ag(rw, WS_ML + (8 * i + (lane >> 3)) * MLH + (lane & 7) * MLS), tag);
          ok = __all(ok);
          if (!ok) __builtin_amdgcn_s_sleep(32);
        }
        if (ok && lane == 0) __hip_atomic_store(reinterpret_cast<u64*>(ws + WS_EPOCH), *reinterpret_cast<const u64*>(&ctl[0]), __ATOMIC_RELAXED, __HIP_MEMORY_SCOPE_AGENT);
      }
      if (proj) {
        ROLES();
        const unsigned xw = __builtin_amdgcn_readfirstlane(ctl[14]), tml = tstart + (xw >> 8) + 10 * (xw & 255u);
        while ((unsigned)__builtin_amdgcn_s_memrealtime() - tml < (unsigned)sched(24) + TOFS + MLD + 30) __builtin_amdgcn_s_sleep(8);
        u32x4 g0;
#pragma unroll 1
        for (int it = 0; it < SPIN_MAX; ++it) {
          FENCE();
          g0 = ld_ag(rw, WS_ML + grp * MLH + (2 * (lane & 3) + ((lane >> 2) & 1)) * MLS);
          if (__all(g_ok(g0, tag))) break;
          __builtin_amdgcn_s_sleep(12);
        }
        const f32x4 a0 = g_val(g0, tag);
        const float mq = (lane & 8) ? a0[2] : a0[0], lq = (lane & 8) ? a0[3] : a0[1];
        float Mx = fmaxf(mq, dpp<XOR1>(mq)); Mx = fmaxf(Mx, dpp<XOR2>(Mx));
        const float e0 = __builtin_amdgcn_exp2f(mq - Mx);
        float lt = e0 * lq; lt += dpp<XOR1>(lt); lt += dpp<XOR2>(lt);
        mlw[2][lane & 15] = e0 / lt;
        LDSINC(ctl[9]);
      }
    }
    ROLES();
    if (w == 0 && ffn) {
      while ((int)((unsigned)__builtin_amdgcn_s_memrealtime() - tstart) < FGT + PHS) __builtin_amdgcn_s_sleep(8);
      const int go = WS_GU + 96 * grp * 8 + 16 * (lane < 48 ? lane : lane & 3);
      const u32x4 stg = s_tag(__builtin_amdgcn_readfirstlane(ctl[0]), lane);
      u32x4 g[4];
      const u32x4 xv = ldb(mkrs(x, grp == 0 ? NB * DM * 2 : 0), ((lane >> 4) * DM + 128 * cb + 8 * (lane & 15)) * 2, 0);
#pragma unroll 1
      for (int it = 0; it < SPIN_MAX; ++it) {
        FENCE();
#pragma unroll
        for (int i = 0; i < 4; ++i) g[i] = ld_ag(rw, go + i * FF * 8);
        if (__all(((s_dif(g[0], stg) | s_dif(g[1], stg) | s_dif(g[2], stg) | s_dif(g[3], stg)) & 15u) == 0u)) break;
        __builtin_amdgcn_s_sleep(32);
      }
      float hv[2][NB];
#pragma unroll
      for (int k = 0; k < 2; ++k) {
        float sc4[4];
#pragma unroll
        for (int i = 0; i < 4; ++i) sc4[i] = s_scale(g[i][2 * k], g[i][2 * k + 1]);
#pragma unroll
        for (int bb = 0; bb < NB; ++bb) {
          float v[4];
#pragma unroll
          for (int i = 0; i < 4; ++i) v[i] = bb < 2 ? s_hi(g[i][2 * k + bb]) : s_lo(g[i][2 * k + bb - 2]);
          const float gg = rbf(fmaf(v[0], sc4[0], v[2] * sc4[2])), up = rbf(fmaf(v[1], sc4[1], v[3] * sc4[3]));
          hv[k][bb] = rbf(gg / (1.f + __expf(-gg))) * up;
        }
      }
#pragma unroll
      for (int bb = 0; bb < NB; ++bb) oA[bb][lane] = cvtpk(hv[0][bb], hv[1][bb]);
      reinterpret_cast<u32x4*>(&red[0][0][0])[lane] = xv;
    }
    if (w < 3 && (proj || ffn)) {
      LDSWAIT(ctl[8], 8, 2);
      tstart = __builtin_amdgcn_readfirstlane(clk[1]) + sched(31) - PHS;
      while ((int)((unsigned)__builtin_amdgcn_s_memrealtime() - tstart) < (proj ? WT + ((grp * WPH) >> 5) : WTF + ((grp * WPF) >> 4))) __builtin_amdgcn_s_sleep(1);
      const rsrc_t ru = mkrs(proj ? wo + (size_t)(grp * DH + 64 * w) * DM : w2 + (size_t)(96 * grp + 32 * w) * DM, (proj ? 64 : 32) * DM * 2);
      const int vo = 8 * (lane >> 4) * (DM * 2) + 256 * cb + 16 * (lane & 15);
      u32x4 wl[16];
#pragma unroll
      for (int e = 0; e < 8; ++e) wl[e] = ldb_nt(ru, vo, e * (DM * 2));
      SB();
      if (proj) {
        while ((int)((unsigned)__builtin_amdgcn_s_memrealtime() - tstart) < WB + ((grp * WPB) >> 5)) __builtin_amdgcn_s_sleep(1);
        SB();
#pragma unroll
        for (int e = 0; e < 8; ++e) wl[8 + e] = ldb_nt(ru, vo, (32 + e) * (DM * 2));
        SB();
#pragma unroll
        for (int ec = 0; ec < 8; ++ec) wf[w][ec][lane] = bfrag(&wl[0], ec);
        LDSINC(ctl[15]);
        SB();
        {
          typedef __attribute__((address_space(3))) u32x2* lw2;
          const unsigned hw = (unsigned)(uintptr_t)&wf[3 + w][0][0] + 8 * lane;
          unsigned b2[8];
#pragma unroll
          for (int ec = 0; ec < 8; ++ec) *reinterpret_cast<lw2>(hw + 1024 * ec) = u32x2{bfrag(&wl[8], ec)[0], bfrag(&wl[8], ec)[1]};
          SB();
#pragma unroll
          for (int ec = 0; ec < 8; ++ec) { b2[ec] = bfrag(&wl[8], ec)[2]; asm volatile("" : "+v"(b2[ec])); }
          SB();
#pragma unroll
          for (int ec = 0; ec < 8; ++ec) *reinterpret_cast<lw2>(hw + 1024 * ec + 512) = u32x2{b2[ec], bfrag(&wl[8], ec)[3]};
        }
      } else {
#pragma unroll
        for (int ec = 0; ec < 8; ++ec) wf[w][ec][lane] = bfrag(&wl[0], ec);
        LDSINC(ctl[15]);
      }
    }
    LDSINC(ctl[6]);
    if (proj && w < 3) BAR();
  }
}

extern "C" void kernel_launch(void* const* d_in, const int* in_sizes, int n_in, void* d_out, int out_size, void* d_ws,
                              size_t ws_size, hipStream_t stream) {
  fused_k<<<256, 768, 0, stream>>>((const bf16*)d_in[0], (const bf16*)d_in[1], (const bf16*)d_in[2], (const bf16*)d_in[3],
                                   (const bf16*)d_in[4], (const bf16*)d_in[6], (const bf16*)d_in[7], (const bf16*)d_in[8],
                                   (unsigned char*)d_ws, (bf16*)d_out);
}
```
